```python
import torch

def _softmax(x, axis=-1):
    return torch.softmax(x, dim=axis)
def _gelu(x):
    return torch.nn.functional.gelu(x, approximate="tanh")
def _silu(x):
    return torch.nn.functional.silu(x)
def _sigmoid(x):
    return torch.sigmoid(x)
def _layernorm(x, axis=-1, eps=1e-5):
    m = x.mean(dim=axis, keepdim=True)
    v = x.var(dim=axis, keepdim=True, unbiased=False)
    return (x - m) / torch.sqrt(v + eps)
def _scatter_add(tgt, idx, src, axis=0):
    return tgt.clone().index_add_(axis, idx, src)
def _segment_sum(src, seg, n):
    out = torch.zeros((n,) + src.shape[1:], dtype=src.dtype)
    return out.index_add_(0, seg, src)
def _pad(x, pw):
    flat = tuple(v for pair in reversed(pw) for v in pair)
    return torch.nn.functional.pad(x, flat)

kernel_name = 'concat_heads__spectral_gfnet__ssm_conv_s4__prompt'

def setup_inputs():

    _w = (lambda a: (lambda t: t.to(torch.bfloat16) if t.is_floating_point() else t)(torch.as_tensor(a)))
    def _A_setup():
        B, S, D = 1, 2048, 640
        Df = D // 2 + 1
        return {
            "x": _w((lambda *s: torch.randn(*s, dtype=torch.bfloat16) * 0.05)(B, S, D)),
            "w_r": _w((lambda *s: torch.randn(*s, dtype=torch.bfloat16) * 0.8)(S, Df) * 0.02),
            "w_i": _w((lambda *s: torch.randn(*s, dtype=torch.bfloat16) * 1.6)(S, Df) * 0.02),
        }
    _A_in = _A_setup()

    def _B_setup():
        B, L, D, N = 1, 2048, 640, 128
        A_re = -(lambda _x: torch.exp(torch.as_tensor(_x)))((lambda *s: torch.randn(*s, dtype=torch.bfloat16) * 3.2)(D, N))
        A_im = torch.pi * (lambda *s: torch.randn(*s, dtype=torch.bfloat16) * 0.05)(D, N)
        B_re = (lambda *s: torch.randn(*s, dtype=torch.bfloat16) * 0.1)(D, N)
        B_im = (lambda *s: torch.randn(*s, dtype=torch.bfloat16) * 0.1)(D, N)
        C_re = (lambda *s: torch.randn(*s, dtype=torch.bfloat16) * 0.1)(D, N)
        C_im = (lambda *s: torch.randn(*s, dtype=torch.bfloat16) * 0.1)(D, N)
        log_dt = (lambda *s: torch.randn(*s, dtype=torch.bfloat16) * 0.05)(D) * 0.5 - 4.0
        Dskip = (lambda *s: torch.randn(*s, dtype=torch.bfloat16) * 0.05)(D)
        return {
            "u": _w((lambda *s: torch.randn(*s, dtype=torch.bfloat16) * 0.05)(B, L, D)),
            "A_re": _w(A_re),
            "A_im": _w(A_im),
            "B_re": _w(B_re),
            "B_im": _w(B_im),
            "C_re": _w(C_re),
            "C_im": _w(C_im),
            "log_dt": _w(log_dt),
            "Dskip": _w(Dskip),
        }
    _B_in = _B_setup()

    return {
        "A_x": _A_in["x"],
        "A_w_r": _A_in["w_r"],
        "A_w_i": _A_in["w_i"],
        "B_u": _B_in["u"],
        "B_A_re": _B_in["A_re"],
        "B_A_im": _B_in["A_im"],
        "B_B_re": _B_in["B_re"],
        "B_B_im": _B_in["B_im"],
        "B_C_re": _B_in["C_re"],
        "B_C_im": _B_in["C_im"],
        "B_log_dt": _B_in["log_dt"],
        "B_Dskip": _B_in["Dskip"],
    }

def reference(A_x, A_w_r, A_w_i, B_u, B_A_re, B_A_im, B_B_re, B_B_im, B_C_re, B_C_im, B_log_dt, B_Dskip):
    def _A_ref(x, w_r, w_i):
        B, S, D = x.shape
        X = (lambda x, axes=(-2,-1): torch.fft.rfft2(x.float(), dim=axes))(x, axes=(-2, -1))
        W = w_r + 1j * w_i
        X = X * W
        y = (lambda x, s, axes=(-2,-1): torch.fft.irfft2(x, s=s, dim=axes).to(torch.bfloat16))(X, (S, D), axes=(-2, -1))
        return y

    def _B_ref(u, A_re, A_im, B_re, B_im, C_re, C_im, log_dt, Dskip):
        Bsz, L, D = u.shape
        dt = torch.exp(log_dt)[:, None]
        A = A_re + 1j * A_im
        Bc = B_re + 1j * B_im
        Cc = C_re + 1j * C_im
        logdA = dt * A
        dB = dt * Bc
        l = (lambda x, dt: x.to(dt))(torch.arange(L), A_re.dtype)
        P = torch.exp(logdA[:, :, None] * l[None, None, :])
        K = torch.einsum("dn,dnl,dn->dl", Cc, P, dB).real
        L2 = 2 * L
        Kp = _pad(K, ((0, 0), (0, L)))
        up = (lambda x, *d: x.permute(*d))(u, 0, 2, 1)
        up = _pad(up, ((0, 0), (0, 0), (0, L)))
        Kf = (lambda x, axes=(-2,-1): torch.fft.rfft2(x.float(), dim=axes))(Kp[:, None, :], axes=(-2, -1))
        Uf = (lambda x, axes=(-2,-1): torch.fft.rfft2(x.float(), dim=axes))(up[:, :, None, :], axes=(-2, -1))
        Yf = Uf * Kf[None, :, :, :]
        y = (lambda x, s, axes=(-2,-1): torch.fft.irfft2(x, s=s, dim=axes).to(torch.bfloat16))(Yf, (1, L2), axes=(-2, -1))[:, :, 0, :L]
        y = (lambda x, *d: x.permute(*d))(y, 0, 2, 1) + u * Dskip[None, None, :]
        return y

    a = _A_ref(A_x, A_w_r, A_w_i)
    b = _B_ref(B_u, B_A_re, B_A_im, B_B_re, B_B_im, B_C_re, B_C_im, B_log_dt, B_Dskip)
    return torch.concatenate([a, b], axis=-1)
```

```cpp
#include <hip/hip_runtime.h>
#include <stdint.h>

#define DI __device__ __forceinline__
typedef __bf16 bf8 __attribute__((ext_vector_type(8)));
typedef __bf16 bf2 __attribute__((ext_vector_type(2)));
typedef float f4 __attribute__((ext_vector_type(4)));
typedef float f2 __attribute__((ext_vector_type(2)));
typedef unsigned u4 __attribute__((ext_vector_type(4)));
typedef unsigned u2 __attribute__((ext_vector_type(2)));
typedef short s4 __attribute__((ext_vector_type(4)));
typedef _Float16 h2 __attribute__((ext_vector_type(2)));
typedef unsigned short u16;
#define LDS3 __attribute__((address_space(3)))
#define GLB1 __attribute__((address_space(1)))

constexpr int S = 2048, D = 640, NS = 128;

constexpr size_t WS_X1 = 0;
constexpr size_t WS_X2 = WS_X1 + 88 * 32768;
constexpr size_t WS_WR = WS_X2 + 352 * 8192;
constexpr size_t WS_WI = WS_WR + 321 * 4096;
constexpr size_t WS_UT = WS_WI + 321 * 4096;
constexpr size_t WS_YT = WS_UT + 640 * 4096;
constexpr size_t WS_KR = WS_YT + 640 * 4096;
constexpr int KR_PITCH = 2080;

constexpr double PI_ = 3.14159265358979323846264338327950288;
constexpr double c_cos_small(double x) { double t = 1, s = 1, x2 = x * x; for (int k = 1; k <= 11; ++k) { t *= -x2 / ((2 * k - 1) * (2 * k)); s += t; } return s; }
constexpr double c_sin_small(double x) { double t = x, s = x, x2 = x * x; for (int k = 1; k <= 11; ++k) { t *= -x2 / ((2 * k) * (2 * k + 1)); s += t; } return s; }
constexpr double c_cos(long m, long n) {
    m %= n; if (m < 0) m += n;
    if (2 * m > n) m = n - m;
    if (4 * m > n) return -c_cos(n - 2 * m, 2 * n);
    if (8 * m > n) return c_sin_small(2 * PI_ * (double)(n - 4 * m) / (double)(4 * n));
    return c_cos_small(2 * PI_ * (double)m / (double)n);
}
constexpr double c_sin(long m, long n) { return c_cos(4 * m - n, 4 * n); }
constexpr u16 c_bf16(double v) { float f = (float)v; unsigned u = __builtin_bit_cast(unsigned, f); u += 0x7fffu + ((u >> 16) & 1u); return (u16)(u >> 16); }
constexpr u16 c_f16(double v) { return __builtin_bit_cast(u16, (_Float16)v); }

template <int N> struct CosTab { double c[N]; constexpr CosTab() : c() { for (int i = 0; i < N; ++i) c[i] = c_cos(i, N); } };
constexpr CosTab<640> C640;
constexpr CosTab<2048> C2048;
constexpr double cs640(int m) { return C640.c[((m % 640) + 640) % 640]; }
constexpr double sn640(int m) { return cs640(m - 160); }
constexpr double cs2048(int m) { return C2048.c[((m % 2048) + 2048) % 2048]; }
constexpr double sn2048(int m) { return cs2048(m - 512); }

struct alignas(16) TF20 { u16 v[2][64][8]; constexpr TF20() : v() {
    for (int c = 0; c < 2; ++c) for (int l = 0; l < 64; ++l) for (int j = 0; j < 8; ++j) { int n1 = 8 * (l >> 4) + j, k1 = l & 15;
        v[c][l][j] = (n1 < 20 && k1 <= 10) ? c_bf16(c == 0 ? cs640(32 * n1 * k1) : -sn640(32 * n1 * k1)) : (u16)0; } } };
struct alignas(16) TE32N { u16 v[2][2][64][8]; constexpr TE32N() : v() {
    for (int t = 0; t < 2; ++t) for (int c = 0; c < 2; ++c) for (int l = 0; l < 64; ++l) for (int j = 0; j < 8; ++j) { int k = 8 * (l >> 4) + j, o = 16 * t + (l & 15);
        v[t][c][l][j] = c_bf16(c == 0 ? cs2048(64 * k * o) : sn2048(64 * k * o)); } } };
struct alignas(16) TE32P { u16 v[2][2][64][8]; constexpr TE32P() : v() {
    for (int t = 0; t < 2; ++t) for (int c = 0; c < 2; ++c) for (int l = 0; l < 64; ++l) for (int j = 0; j < 8; ++j) { int k = 16 * (j >> 2) + 4 * (l >> 4) + (j & 3), o = 16 * t + (l & 15);
        v[t][c][l][j] = c_bf16(c == 0 ? cs2048(64 * k * o) : sn2048(64 * k * o)); } } };
struct alignas(16) TF64 { u16 v[4][2][2][64][8]; constexpr TF64() : v() {
    for (int w = 0; w < 4; ++w) for (int c = 0; c < 2; ++c) for (int s = 0; s < 2; ++s) for (int l = 0; l < 64; ++l) for (int j = 0; j < 8; ++j) { int k = 32 * s + 8 * (l >> 4) + j, o = 16 * w + (l & 15);
        v[w][c][s][l][j] = c_bf16(c == 0 ? cs2048(32 * k * o) : sn2048(32 * k * o)); } } };
struct alignas(16) TG20 { u16 v[2][64][8]; constexpr TG20() : v() {
    for (int t = 0; t < 2; ++t) for (int l = 0; l < 64; ++l) for (int j = 0; j < 8; ++j) { int slot = 8 * (l >> 4) + j, k1 = slot >> 1, c = slot & 1, n1 = 16 * t + (l & 15);
        double w = (k1 == 0 || k1 == 10) ? 1.0 : 2.0;
        v[t][l][j] = (k1 <= 10 && n1 < 20) ? c_bf16(w / (2048.0 * 640.0) * (c == 0 ? cs640(32 * n1 * k1) : -sn640(32 * n1 * k1))) : (u16)0; } } };
struct alignas(16) TTW640A { u16 v[64][8][2]; constexpr TTW640A() : v() {
    for (int l = 0; l < 64; ++l) for (int e = 0; e < 8; ++e) { int n2 = 8 * (l >> 4) + e, k1 = l & 15; v[l][e][0] = c_f16(cs640(n2 * k1)); v[l][e][1] = c_f16(-sn640(n2 * k1)); } } };
struct alignas(16) TTW1 { u16 v[4][64][8][2]; constexpr TTW1() : v() {
    for (int w = 0; w < 4; ++w) for (int l = 0; l < 64; ++l) for (int e = 0; e < 8; ++e) { int n2 = 16 * (e >> 2) + 4 * (l >> 4) + (e & 3), k1 = 16 * w + (l & 15);
        v[w][l][e][0] = c_f16(cs2048(n2 * k1)); v[w][l][e][1] = c_f16(-sn2048(n2 * k1)); } } };
struct alignas(16) TTW2 { u16 v[4][64][8][2]; constexpr TTW2() : v() {
    for (int w = 0; w < 4; ++w) for (int l = 0; l < 64; ++l) for (int e = 0; e < 8; ++e) { int k1 = 16 * w + 4 * (l >> 4) + (e & 3), n2 = 16 * (e >> 2) + (l & 15);
        v[w][l][e][0] = c_f16(cs2048(n2 * k1)); v[w][l][e][1] = c_f16(sn2048(n2 * k1)); } } };
struct alignas(16) TTW3 { u16 v[2][6][64][4][2]; constexpr TTW3() : v() {
    for (int t = 0; t < 2; ++t) for (int p = 0; p < 6; ++p) for (int l = 0; l < 64; ++l) for (int r = 0; r < 4; ++r) { int n2 = 16 * t + 4 * (l >> 4) + r, k1 = 2 * p + ((l & 15) >> 3);
        v[t][p][l][r][0] = c_f16(cs640(n2 * k1)); v[t][p][l][r][1] = c_f16(sn640(n2 * k1)); } } };
struct alignas(16) TGMAP { u16 v[2048]; constexpr TGMAP() : v() {
    for (int x = 0; x < 2048; ++x) { float f = (float)(2047 - x); unsigned u = __builtin_bit_cast(unsigned, f); u = (u + 0x7fffu + ((u >> 16) & 1u)) & 0xffff0000u;
        v[x] = (u16)(2 * (int)__builtin_bit_cast(float, u)); } } };
__device__ const TF20 tF20 = TF20();
__device__ const TE32N tE32N = TE32N();
__device__ const TE32P tE32P = TE32P();
__device__ const TF64 tF64 = TF64();
__device__ const TG20 tG20 = TG20();
__device__ const TTW640A tTW640A = TTW640A();
__device__ const TTW1 tTW1 = TTW1();
__device__ const TTW2 tTW2 = TTW2();
__device__ const TTW3 tTW3 = TTW3();
__device__ const TGMAP tGMAP = TGMAP();

DI f4 mfma(bf8 a, bf8 b, f4 c) { return __builtin_amdgcn_mfma_f32_16x16x32_bf16(a, b, c, 0, 0, 0); }
DI unsigned pk(float a, float b) { f2 x = {a, b}; return __builtin_bit_cast(unsigned, __builtin_convertvector(x, bf2)); }
DI bf8 pk8(f4 a, f4 b) { u4 r = {pk(a[0], a[1]), pk(a[2], a[3]), pk(b[0], b[1]), pk(b[2], b[3])}; return __builtin_bit_cast(bf8, r); }
DI bf8 neg8(bf8 a) { u4 r = __builtin_bit_cast(u4, a); r ^= 0x80008000u; return __builtin_bit_cast(bf8, r); }
DI bf8 ld8(const void* p) { return __builtin_bit_cast(bf8, *(const u4*)p); }
DI float bf2f(u16 v) { return __builtin_bit_cast(float, (unsigned)v << 16); }
DI float lo16(unsigned v) { return __builtin_bit_cast(float, v << 16); }
DI float hi16(unsigned v) { return __builtin_bit_cast(float, v & 0xffff0000u); }
DI void dma16(const void* g, void* lds) { __builtin_amdgcn_global_load_lds((const GLB1 void*)g, (LDS3 void*)lds, 16, 0, 0); }
DI s4 trd(const void* lds) { return __builtin_amdgcn_ds_read_tr16_b64_v4i16((LDS3 s4*)lds); }
DI bf8 cat8(s4 a, s4 b) { return __builtin_bit_cast(bf8, __builtin_shufflevector(a, b, 0, 1, 2, 3, 4, 5, 6, 7)); }
DI void wave_lds_fence() { __builtin_amdgcn_fence(__ATOMIC_RELEASE, "wavefront"); __builtin_amdgcn_s_waitcnt(0xC07F); __builtin_amdgcn_fence(__ATOMIC_ACQUIRE, "wavefront"); }
DI void wait_vm0() { asm volatile("s_waitcnt vmcnt(0)" ::: "memory"); }
DI void cmul_h(float& x, float& y, unsigned cs) { h2 h = __builtin_bit_cast(h2, cs); float c = (float)h[0], s = (float)h[1]; float nx = x * c - y * s, ny = y * c + x * s; x = nx; y = ny; }
DI f2 cmul(f2 a, f2 b) { f2 t = a * b[0]; f2 sw = {-a[1], a[0]}; return t + sw * b[1]; }

template <int NC>
DI void transpose_wave(const char* src, size_t sp, char* dst, size_t dp, char* lds, int lane) {
    constexpr int CH = NC / 8;
    constexpr int RP = CH * 16;
    constexpr int IT = CH;
#pragma unroll
    for (int it = 0; it < IT; ++it) { int id = it * 64 + lane, row = id / CH, ch = id % CH; dma16(src + row * sp + ch * 16, lds + it * 1024); }
    wait_vm0();
    u4 item[IT];
    const int G = lane >> 4, q = (lane & 15) >> 2, p = lane & 3;
#pragma unroll
    for (int it = 0; it < IT; ++it) {
        int pi = it * 16 + 4 * G + p, quad = pi % (NC / 4), oct = pi / (NC / 4);
        const char* a = lds + (8 * oct + q) * RP + quad * 8;
        s4 lo = trd(a), hi = trd(a + 4 * RP);
        item[it] = __builtin_bit_cast(u4, cat8(lo, hi));
    }
    wave_lds_fence();
#pragma unroll
    for (int it = 0; it < IT; ++it) {
        int pi = it * 16 + 4 * G + q, quad = pi % (NC / 4), oct = pi / (NC / 4), col = 4 * quad + p;
        *(u4*)(lds + col * 128 + ((oct ^ (col & 7)) * 16)) = item[it];
    }
    wave_lds_fence();
#pragma unroll
    for (int it = 0; it < IT; ++it) { int id = it * 64 + lane, col = id >> 3, oct = id & 7;
        u4 v = *(const u4*)(lds + col * 128 + ((oct ^ (col & 7)) * 16));
        *(u4*)(dst + col * dp + oct * 16) = v; }
}

constexpr int K1_KGEN = 320, K1_ROW = 256, K1_TR = 256;
constexpr int K1_LDS = 26624;

DI void k1_kgen(int b, const u16* Are, const u16* Aim, const u16* Bre, const u16* Bim, const u16* Cre, const u16* Cim, const u16* logdt, char* ws, char* lds) {
    const int tid = threadIdx.x, lane = tid & 63, w = __builtin_amdgcn_readfirstlane(tid >> 6);
    const int d = 2 * b + (w >> 1), n = 64 * (w & 1) + lane, idx = d * NS + n;
    const u4* gm = (const u4*)(tGMAP.v) + 2 * (tid & 127);
    const u4 gm0 = gm[0], gm1 = gm[1];
    const float are = bf2f(Are[idx]), aim = bf2f(Aim[idx]), bre = bf2f(Bre[idx]), bim = bf2f(Bim[idx]), cre = bf2f(Cre[idx]), cim = bf2f(Cim[idx]);
    const float dt = lo16(pk(expf(bf2f(logdt[d])), 0.f));
    const float er = (dt * are) * 1.44269504088896340736f, th = (dt * aim) * 0.15915494309189533577f;
    const f2 CB = cmul(f2{cre, cim}, f2{dt * bre, dt * bim});
    auto direct = [&](float v) { float e = __builtin_amdgcn_exp2f(er * v), t = __builtin_amdgcn_fractf(th * v); return f2{e * __builtin_amdgcn_cosf(t), e * __builtin_amdgcn_sinf(t)}; };
    const f2 z1 = direct(1.f), z8 = direct(8.f), z128 = direct(128.f);
    constexpr int SE = 272;
    char* tb = lds + w * (16 * SE);
    auto putQ = [&](int e, f2 v) { *(unsigned*)(tb + e * SE + lane * 4) = pk(v[0], v[1]); };
    auto putP = [&](int e, f2 v) { *(unsigned*)(tb + e * SE + lane * 4) = pk(v[0], -v[1]); };
    const char* rd = tb + (lane & 15) * SE + (lane >> 4) * 16;
    bf8 qf[4];
    {
        f2 q = z8; putQ(0, f2{1.f, 0.f}); putQ(1, q);
#pragma unroll
        for (int j = 2; j < 16; ++j) { q = cmul(q, z8); putQ(j, q); }
        wave_lds_fence();
#pragma unroll
        for (int s = 0; s < 4; ++s) qf[s] = ld8(rd + s * 64);
        wave_lds_fence();
    }
    f4 acc[3];
    auto tile = [&](int t) { wave_lds_fence(); f4 a = {0, 0, 0, 0};
#pragma unroll
        for (int s = 0; s < 4; ++s) a = mfma(ld8(rd + s * 64), qf[s], a);
        acc[t] = a; wave_lds_fence(); };
    f2 F[8]; F[0] = CB;
#pragma unroll
    for (int r = 1; r < 8; ++r) F[r] = cmul(F[r - 1], z1);
#pragma unroll
    for (int r = 0; r < 8; ++r) { putP(r, F[r]); putP(8 + r, cmul(F[r], z128)); }
    tile(0);
    const f2 z256 = cmul(z128, z128), z384 = cmul(z256, z128), z512 = cmul(z256, z256), z640 = cmul(z512, z128), z768 = cmul(z512, z256), z896 = cmul(z768, z128), z1024 = cmul(z512, z512);
#pragma unroll
    for (int r = 0; r < 4; ++r) { putP(r, cmul(F[2 * r], z256)); putP(4 + r, cmul(F[2 * r], z384)); }
#pragma unroll
    for (int r = 0; r < 2; ++r) { putP(8 + r, cmul(F[4 * r], z512)); putP(10 + r, cmul(F[4 * r], z640)); putP(12 + r, cmul(F[4 * r], z768)); putP(14 + r, cmul(F[4 * r], z896)); }
    tile(1);
    { f2 g = cmul(CB, z1024); putP(0, g);
#pragma unroll
      for (int i = 1; i < 9; ++i) { g = cmul(g, z128); putP(i, g); } }
    tile(2);
    char* red = lds + 4 * 16 * SE + (w >> 1) * 3072;
    char* kd = lds + 4 * 16 * SE + 2 * 3072 + (w >> 1) * 4352;
    if (w & 1) {
#pragma unroll
        for (int t = 0; t < 3; ++t) *(f4*)(red + t * 1024 + lane * 16) = acc[t];
    }
    __syncthreads();
    if (!(w & 1)) {
        const int g = lane >> 4, j = lane & 15;
#pragma unroll
        for (int t = 0; t < 3; ++t) { const f4 o = *(const f4*)(red + t * 1024 + lane * 16);
#pragma unroll
            for (int r = 0; r < 4; ++r) { const int i = 4 * g + r;
                int p = (t == 0) ? (i & 7) + 128 * (i >> 3) : (t == 1) ? (i < 8 ? 256 + 128 * (i >> 2) + 2 * (i & 3) : 512 + 128 * ((i - 8) >> 1) + 4 * (i & 1)) : 1024 + 128 * i;
                if (t < 2 || i < 9) *(u16*)(kd + 2 * (p + 8 * j)) = (u16)pk(acc[t][r] + o[r], 0.f); } }
    }
    __syncthreads();
    {
        const int t = tid & 127;
        u4 o0, o1;
#pragma unroll
        for (int k = 0; k < 4; ++k) {
            o0[k] = *(const u16*)(kd + (gm0[k] & 0xffff)) | ((unsigned)*(const u16*)(kd + (gm0[k] >> 16)) << 16);
            o1[k] = *(const u16*)(kd + (gm1[k] & 0xffff)) | ((unsigned)*(const u16*)(kd + (gm1[k] >> 16)) << 16);
        }
        char* kr = ws + WS_KR + (size_t)d * (KR_PITCH * 2);
        *(u4*)(kr + t * 32) = o0; *(u4*)(kr + t * 32 + 16) = o1;
        if (t < 4) *(u4*)(kr + 4096 + t * 16) = u4{0, 0, 0, 0};
    }
}

DI void k1_row(int rb, const char* x, char* ws, char* lds) {
    const int tid = threadIdx.x, lane = tid & 63, w = __builtin_amdgcn_readfirstlane(tid >> 6);
    const int n2s = rb & 31, n1g = rb >> 5, g = lane >> 4, i = lane & 15;
    char* xs = lds + w * 2560;
#pragma unroll
    for (int it = 0; it < 3; ++it) { int id = it * 64 + lane; if (id < 160) { int rr = id / 80, ch = id % 80; int s = 32 * (8 * n1g + 2 * w + rr) + n2s; dma16(x + (size_t)s * 1280 + ch * 16, xs + it * 1024); } }
    const bf8 fc = ld8(tF20.v[0][lane]), fs = ld8(tF20.v[1][lane]);
    const u4 tw0 = *(const u4*)tTW640A.v[lane][0], tw1 = *(const u4*)tTW640A.v[lane][4];
    const int kt = w & 1;
    const bf8 ec = ld8(tE32N.v[kt][0][lane]), es = ld8(tE32N.v[kt][1][lane]), en = neg8(es);
    wait_vm0();
    constexpr int SR = 128, SK = 1072;
    char* ex = lds + 10240;
    const int q = i >> 2, p = i & 3;
#pragma unroll
    for (int rr = 0; rr < 2; ++rr) {
        f4 dr[2], di[2];
#pragma unroll
        for (int t = 0; t < 2; ++t) {
            int n1a = 8 * g + q, n1b = n1a + 4; n1a = n1a < 20 ? n1a : 19; n1b = n1b < 20 ? n1b : 19;
            const char* base = xs + rr * 1280 + 16 * p + 8 * t;
            bf8 a = cat8(trd(base + 64 * n1a), trd(base + 64 * n1b));
            dr[t] = mfma(a, fc, f4{0, 0, 0, 0}); di[t] = mfma(a, fs, f4{0, 0, 0, 0});
#pragma unroll
            for (int r = 0; r < 4; ++r) { float xr = dr[t][r], xi = di[t][r]; cmul_h(xr, xi, t ? tw1[r] : tw0[r]); dr[t][r] = xr; di[t][r] = xi; }
        }
        if (i < 12) { char* o = ex + i * SK + (2 * w + rr) * SR + g * 16; *(bf8*)o = pk8(dr[0], dr[1]); *(bf8*)(o + 64) = pk8(di[0], di[1]); }
    }
    __syncthreads();
#pragma unroll
    for (int pp = 0; pp < 3; ++pp) {
        const int pr = 3 * (w >> 1) + pp;
        const char* a = ex + (2 * pr + (i >> 3)) * SK + (i & 7) * SR + g * 16;
        const bf8 ar = ld8(a), ai = ld8(a + 64);
        f4 xr = mfma(ar, ec, f4{0, 0, 0, 0}); xr = mfma(ai, es, xr);
        f4 xi = mfma(ai, ec, f4{0, 0, 0, 0}); xi = mfma(ar, en, xi);
        auto s0 = __builtin_amdgcn_permlane16_swap(pk(xr[0], xr[1]), pk(xi[0], xi[1]), false, false);
        auto s1 = __builtin_amdgcn_permlane16_swap(pk(xr[2], xr[3]), pk(xi[2], xi[3]), false, false);
        const int k1 = 2 * pr + (g >> 1), kap = 32 * k1 + 16 * kt + i;
        if (k1 <= 10) *(u4*)(ws + WS_X1 + (size_t)(kap >> 2) * 32768 + (n2s * 8 + n1g) * 128 + (kap & 3) * 32 + (g & 1) * 16) = u4{s0[0], s1[0], s0[1], s1[1]};
    }
}

DI void k1_tr(int b, const char* u, const char* wr, const char* wi, char* ws, char* lds) {
    const int lane = threadIdx.x & 63, w = __builtin_amdgcn_readfirstlane(threadIdx.x >> 6);
    char* my = lds + w * 5120;
    const int xcd = b & 7, lt = b >> 3;
    if (w < 2) {
        const int cg = 2 * xcd + w;
        transpose_wave<40>(u + (size_t)lt * 64 * 1280 + cg * 80, 1280, ws + WS_UT + (size_t)cg * 40 * 4096 + lt * 128, 4096, my, lane);
    } else {
        const char* src = (w == 2) ? wr : wi; char* dst = ws + (w == 2 ? WS_WR : WS_WI);
        u16 last = 0;
        if (xcd == 7) last = *(const u16*)(src + (size_t)(lt * 64 + lane) * 642 + 640);
        transpose_wave<40>(src + (size_t)lt * 64 * 642 + xcd * 80, 642, dst + (size_t)xcd * 40 * 4096 + lt * 128, 4096, my, lane);
        if (xcd == 7) *(u16*)(dst + (size_t)320 * 4096 + lt * 128 + lane * 2) = last;
    }
}

struct P1 { const char *x, *wr, *wi, *u; const u16 *Are, *Aim, *Bre, *Bim, *Cre, *Cim, *logdt; char* ws; };
__global__ void __launch_bounds__(256) k1(P1 p) {
    __shared__ __attribute__((aligned(16))) char lds[2 * 3072 + 2 * 4352 + 4 * 16 * 272 > 10240 + 12 * 1072 ? 2 * 3072 + 2 * 4352 + 4 * 16 * 272 : 10240 + 12 * 1072];
    const int b = blockIdx.x;
    if (b < K1_KGEN) k1_kgen(b, p.Are, p.Aim, p.Bre, p.Bim, p.Cre, p.Cim, p.logdt, p.ws, lds);
    else if (b < K1_KGEN + K1_ROW) k1_row(b - K1_KGEN, p.x, p.ws, lds);
    else k1_tr(b - K1_KGEN - K1_ROW, p.u, p.wr, p.wi, p.ws, lds);
}

constexpr int K2_CONV = 640, K2_COL = 352;
constexpr int KCP = 4160;

DI void k2_conv(int d, const u16* dskip, char* ws, char* lds) {
    const int tid = threadIdx.x, lane = tid & 63, w = __builtin_amdgcn_readfirstlane(tid >> 6), g = lane >> 4, i = lane & 15;
    char* ks = lds;
    char* us = lds + 4 * KCP;
    {
        const char* kr = ws + WS_KR + (size_t)d * (KR_PITCH * 2) + w * 2 + lane * 16;
#pragma unroll
        for (int it = 0; it < 4; ++it) dma16(kr + it * 1024, ks + w * KCP + it * 1024);
        dma16(ws + WS_UT + (size_t)d * 4096 + w * 1024 + lane * 16, us + 512 + w * 1024);
        if (lane < 4) *(u4*)(ks + w * KCP + 4096 + lane * 16) = u4{0, 0, 0, 0};
        if (tid < 32) *(u4*)(us + tid * 16) = u4{0, 0, 0, 0};
    }
    const float dsk = bf2f(dskip[d]);
    wait_vm0();
    __syncthreads();
    f4 acc[8];
#pragma unroll
    for (int c = 0; c < 8; ++c) acc[c] = f4{0, 0, 0, 0};
    const int sh = (3 - i) & 3;
#pragma unroll 1
    for (int sg = 0; sg < 2; ++sg) {
        const int mu = 4 * w + 2 * sg;
        const int x0 = 2047 - 16 * mu - 16 * (g >> 1) + 8 * (g & 1) - i;
        const char* ap = ks + sh * KCP + 2 * (x0 - sh);
        const char* bp = us + 512 + 32 * (i - mu - (g >> 1)) + 16 * (g & 1);
        bf8 B[8];
#pragma unroll
        for (int j = 0; j < 8; ++j) B[j] = ld8(bp + 512 * j);
#pragma unroll
        for (int q = 0; q < 8; ++q) {
            const u2 a0 = *(const u2*)(ap - 512 * q), a1 = *(const u2*)(ap - 512 * q + 8);
            const bf8 A = __builtin_bit_cast(bf8, u4{a0[0], a0[1], a1[0], a1[1]});
#pragma unroll
            for (int c = q; c < 8; ++c) acc[c] = mfma(A, B[c - q], acc[c]);
        }
    }
    u2 uu[2];
#pragma unroll
    for (int h = 0; h < 2; ++h) uu[h] = *(const u2*)(us + 512 + 2 * (256 * (2 * w + h) + 16 * i + 4 * g));
    __syncthreads();
#pragma unroll
    for (int c = 0; c < 8; ++c) { const int o = c >> 1; if (o != w) { const int slot = (c > 2 * w) ? c - 2 : c; *(f4*)(lds + (w * 6 + slot) * 1024 + lane * 16) = acc[c]; } }
    __syncthreads();
#pragma unroll
    for (int h = 0; h < 2; ++h) {
        f4 y = {0, 0, 0, 0};
#pragma unroll
        for (int c = 0; c < 8; ++c) if (c == 2 * w + h) y = acc[c];
        const int c = 2 * w + h;
#pragma unroll
        for (int sw = 0; sw < 4; ++sw) if (sw != w) { const int slot = (c > 2 * sw) ? c - 2 : c; y += *(const f4*)(lds + (sw * 6 + slot) * 1024 + lane * 16); }
        const unsigned y01 = pk(y[0], y[1]), y23 = pk(y[2], y[3]);
        const unsigned s01 = pk(lo16(uu[h][0]) * dsk, hi16(uu[h][0]) * dsk), s23 = pk(lo16(uu[h][1]) * dsk, hi16(uu[h][1]) * dsk);
        const u2 o = {pk(lo16(y01) + lo16(s01), hi16(y01) + hi16(s01)), pk(lo16(y23) + lo16(s23), hi16(y23) + hi16(s23))};
        *(u2*)(ws + WS_YT + (size_t)d * 4096 + 2 * (256 * c + 16 * i + 4 * g)) = o;
    }
}

DI int mixsw(int r) { return (r & 3) | ((r >> 1) & 4); }

DI void k2_col(int b, char* ws, char* lds) {
    const int xcd = b & 7, y = b >> 3, gam = ((y >> 2) << 3) | xcd, kap = 4 * gam + (y & 3);
    const int k1 = kap >> 5, k2 = kap & 31;
    if ((k1 == 0 && k2 > 16) || (k1 == 10 && k2 > 15)) return;
    const int kfull = k1 + 20 * k2; const bool mir = kfull > 320; const int kc = mir ? 640 - kfull : kfull;
    const int tid = threadIdx.x, lane = tid & 63, w = __builtin_amdgcn_readfirstlane(tid >> 6), g = lane >> 4, i = lane & 15;
    char* xs = lds;
    char* wl = lds + 8192;
    char* ex = lds + 16384;
#pragma unroll
    for (int it = 0; it < 2; ++it) {
        const int slot = (2 * w + it) * 64 + lane, n2 = slot >> 4, uu = (slot & 15) ^ (n2 & 15);
        dma16(ws + WS_X1 + (size_t)gam * 32768 + (n2 * 8 + (uu >> 1)) * 128 + (kap & 3) * 32 + (uu & 1) * 16, xs + (2 * w + it) * 1024);
    }
    dma16(ws + WS_WR + (size_t)kc * 4096 + w * 1024 + lane * 16, wl + w * 1024);
    dma16(ws + WS_WI + (size_t)kc * 4096 + w * 1024 + lane * 16, wl + 4096 + w * 1024);
    const bf8 fc0 = ld8(tF64.v[w][0][0][lane]), fc1 = ld8(tF64.v[w][0][1][lane]), fs0 = ld8(tF64.v[w][1][0][lane]), fs1 = ld8(tF64.v[w][1][1][lane]);
    const bf8 ec0 = ld8(tE32P.v[0][0][lane]), es0 = ld8(tE32P.v[0][1][lane]), ec1 = ld8(tE32P.v[1][0][lane]), es1 = ld8(tE32P.v[1][1][lane]);
    const u4 ta0 = *(const u4*)tTW1.v[w][lane][0], ta1 = *(const u4*)tTW1.v[w][lane][4];
    const u4 tb0 = *(const u4*)tTW2.v[w][lane][0], tb1 = *(const u4*)tTW2.v[w][lane][4];
    wait_vm0();
    __syncthreads();
    const bf8 fn0 = neg8(fs0), fn1 = neg8(fs1), en0 = neg8(es0), en1 = neg8(es1);
    f4 yr[2], yi[2];
#pragma unroll
    for (int t = 0; t < 2; ++t) {
        const int n2 = 16 * t + i; const char* row = xs + n2 * 256;
        auto fr = [&](int ks, int c) { return ld8(row + ((((4 * ks + g) * 2 + c) ^ i) * 16)); };
        const bf8 r0 = fr(0, 0), r1 = fr(1, 0), i0 = fr(0, 1), i1 = fr(1, 1);
        f4 a = mfma(r0, fc0, f4{0, 0, 0, 0}); a = mfma(r1, fc1, a); a = mfma(i0, fs0, a); a = mfma(i1, fs1, a);
        f4 c = mfma(i0, fc0, f4{0, 0, 0, 0}); c = mfma(i1, fc1, c); c = mfma(r0, fn0, c); c = mfma(r1, fn1, c);
#pragma unroll
        for (int r = 0; r < 4; ++r) { float x = a[r], yv = c[r]; cmul_h(x, yv, t ? ta1[r] : ta0[r]); a[r] = x; c[r] = yv; }
        yr[t] = a; yi[t] = c;
    }
    const bf8 Yr = pk8(yr[0], yr[1]), Yi = pk8(yi[0], yi[1]);
    f4 zr[2], zi[2];
    zr[0] = mfma(es0, Yi, mfma(ec0, Yr, f4{0, 0, 0, 0})); zi[0] = mfma(en0, Yr, mfma(ec0, Yi, f4{0, 0, 0, 0}));
    zr[1] = mfma(es1, Yi, mfma(ec1, Yr, f4{0, 0, 0, 0})); zi[1] = mfma(en1, Yr, mfma(ec1, Yi, f4{0, 0, 0, 0}));
#pragma unroll
    for (int t = 0; t < 2; ++t)
#pragma unroll
        for (int r = 0; r < 4; ++r) {
            const int ks = 16 * w + i + 64 * (16 * t + 4 * g + r), pos = mir ? ((2048 - ks) & 2047) : ks;
            const float a = bf2f(*(const u16*)(wl + 2 * pos)); float bq = bf2f(*(const u16*)(wl + 4096 + 2 * pos)); if (mir) bq = -bq;
            const float x = zr[t][r], yv = zi[t][r];
            zr[t][r] = x * a - yv * bq; zi[t][r] = yv * a + x * bq;
        }
    const bf8 Zr = pk8(zr[0], zr[1]), Zi = pk8(zi[0], zi[1]);
#pragma unroll
    for (int tt = 0; tt < 2; ++tt) {
        const bf8 ec = tt ? ec1 : ec0, es = tt ? es1 : es0, en = tt ? en1 : en0;
        f4 vr = mfma(Zi, en, mfma(Zr, ec, f4{0, 0, 0, 0})), vi = mfma(Zr, es, mfma(Zi, ec, f4{0, 0, 0, 0}));
#pragma unroll
        for (int r = 0; r < 4; ++r) { float x = vr[r], yv = vi[r]; cmul_h(x, yv, tt ? tb1[r] : tb0[r]); vr[r] = x; vi[r] = yv; }
        const int row = 16 * tt + i, k = 16 * w + 4 * g;
        char* o = ex + row * 128 + (((k >> 3) ^ mixsw(row)) * 16) + (k & 7) * 2;
        *(u2*)o = u2{pk(vr[0], vr[1]), pk(vr[2], vr[3])}; *(u2*)(o + 4096) = u2{pk(vi[0], vi[1]), pk(vi[2], vi[3])};
    }
    __syncthreads();
    f4 or_[2], oi[2];
#pragma unroll
    for (int tt = 0; tt < 2; ++tt) {
        const int row = 8 * (i >> 2) + 4 * tt + (i & 3); const char* base = ex + row * 128; const int sw = mixsw(row);
        const bf8 r0 = ld8(base + ((g ^ sw) * 16)), r1 = ld8(base + (((4 + g) ^ sw) * 16)), i0 = ld8(base + 4096 + ((g ^ sw) * 16)), i1 = ld8(base + 4096 + (((4 + g) ^ sw) * 16));
        f4 a = mfma(r0, fc0, f4{0, 0, 0, 0}); a = mfma(r1, fc1, a); a = mfma(i0, fn0, a); a = mfma(i1, fn1, a);
        f4 c = mfma(i0, fc0, f4{0, 0, 0, 0}); c = mfma(i1, fc1, c); c = mfma(r0, fs0, c); c = mfma(r1, fs1, c);
        or_[tt] = a; oi[tt] = c;
    }
    u4 vr = __builtin_bit_cast(u4, pk8(or_[0], or_[1])), vi = __builtin_bit_cast(u4, pk8(oi[0], oi[1]));
    if (kfull == 0 || kfull == 320) vi = u4{0, 0, 0, 0};
    char* o = ws + WS_X2 + (size_t)kap * 8192 + g * 2048 + (16 * w + i) * 32;
    *(u4*)o = vr; *(u4*)(o + 16) = vi;
    int kap2 = -1;
    if (k1 == 0 && k2 >= 1 && k2 <= 15) kap2 = 32 - k2; else if (k1 == 10) kap2 = 320 + 31 - k2;
    if (kap2 >= 0) { char* o2 = ws + WS_X2 + (size_t)kap2 * 8192 + g * 2048 + (16 * w + i) * 32; *(u4*)o2 = vr; *(u4*)(o2 + 16) = vi ^ 0x80008000u; }
}

struct P2 { const u16* dskip; char* ws; };
__global__ void __launch_bounds__(256) k2(P2 p) {
    __shared__ __attribute__((aligned(16))) char lds[24576];
    const int b = blockIdx.x;
    if (b < K2_CONV) k2_conv(b, p.dskip, p.ws, lds);
    else k2_col(b - K2_CONV, p.ws, lds);
}

constexpr int K3_ROW = 256, K3_TR = 160;

DI void k3_row(int b, const char* ws, char* out, char* lds) {
    const int xcd = b & 7, y = b >> 3, gq = (xcd + 8 * (y >> 2)), gg = gq & 3, n1s = 4 * (gq >> 2) + (y & 3);
    const int tid = threadIdx.x, lane = tid & 63, w = __builtin_amdgcn_readfirstlane(tid >> 6), g = lane >> 4, i = lane & 15;
    char* xs = lds;
    char* ex = lds + 11264;
    constexpr int SR = 2064;
#pragma unroll
    for (int it = 0; it < 3; ++it) { const int id = (w * 3 + it) * 64 + lane; if (id < 704) dma16(ws + WS_X2 + (size_t)(id >> 1) * 8192 + gg * 2048 + n1s * 32 + (id & 1) * 16, xs + (w * 3 + it) * 1024); }
    const int t = w & 1;
    const bf8 ec = ld8(tE32N.v[t][0][lane]), es = ld8(tE32N.v[t][1][lane]), en = neg8(es);
    const bf8 g0 = ld8(tG20.v[0][lane]), g1 = ld8(tG20.v[1][lane]);
    u2 tw[3][2];
#pragma unroll
    for (int pp = 0; pp < 3; ++pp) { const u4 v = *(const u4*)tTW3.v[t][3 * (w >> 1) + pp][lane][0]; tw[pp][0] = u2{v[0], v[1]}; tw[pp][1] = u2{v[2], v[3]}; }
    { const int rho = tid >> 5, n2 = tid & 31; *(u4*)(ex + rho * SR + n2 * 64 + 48) = u4{0, 0, 0, 0}; }
    wait_vm0();
    __syncthreads();
    const int q = i >> 2, p = i & 3;
#pragma unroll
    for (int pp = 0; pp < 3; ++pp) {
        const int pr = 3 * (w >> 1) + pp;
        int k1 = 2 * pr + (p >> 1); k1 = k1 <= 10 ? k1 : 10;
        const char* a = xs + (32 * k1 + 8 * g + q) * 32 + (p & 1) * 8;
        const bf8 xr = cat8(trd(a), trd(a + 128)), xi = cat8(trd(a + 16), trd(a + 16 + 128));
        f4 vr = mfma(en, xi, mfma(ec, xr, f4{0, 0, 0, 0})), vi = mfma(es, xr, mfma(ec, xi, f4{0, 0, 0, 0}));
        char* o = ex + (i & 7) * SR + (16 * t + 4 * g) * 64 + (2 * pr + (i >> 3)) * 4;
#pragma unroll
        for (int r = 0; r < 4; ++r) { float x = vr[r], yv = vi[r]; cmul_h(x, yv, tw[pp][r >> 1][r & 1]); *(unsigned*)(o + r * 64) = pk(x, yv); }
    }
    __syncthreads();
#pragma unroll
    for (int rr = 0; rr < 2; ++rr) {
        const int rho = 2 * w + rr;
        const bf8 a0 = ld8(ex + rho * SR + (8 * (i >> 2) + (i & 3)) * 64 + g * 16), a1 = ld8(ex + rho * SR + (8 * (i >> 2) + 4 + (i & 3)) * 64 + g * 16);
        const f4 z = {0, 0, 0, 0};
        const bf8 o0 = pk8(mfma(a0, g0, z), mfma(a1, g0, z)), o1 = pk8(mfma(a0, g1, z), mfma(a1, g1, z));
        char* orow = out + (size_t)(32 * n1s + 8 * gg + rho) * 2560;
        *(bf8*)(orow + i * 64 + g * 16) = o0;
        if (i < 4) *(bf8*)(orow + (16 + i) * 64 + g * 16) = o1;
    }
}

DI void k3_tr(int b, const char* ws, char* out, char* lds) {
    const int lane = threadIdx.x & 63, w = __builtin_amdgcn_readfirstlane(threadIdx.x >> 6);
    const int id = b * 4 + w, dt = id % 10, lt = id / 10;
    transpose_wave<32>(ws + WS_YT + (size_t)dt * 64 * 4096 + lt * 64, 4096, out + (size_t)lt * 32 * 2560 + 1280 + dt * 128, 2560, lds + w * 4096, lane);
}

struct P3 { const char* ws; char* out; };
__global__ void __launch_bounds__(256) k3(P3 p) {
    __shared__ __attribute__((aligned(16))) char lds[11264 + 8 * 2064];
    const int b = blockIdx.x;
    if (b < K3_ROW) k3_row(b, p.ws, p.out, lds);
    else k3_tr(b - K3_ROW, p.ws, p.out, lds);
}

extern "C" void kernel_launch(void* const* d_in, const int* in_sizes, int n_in, void* d_out, int out_size, void* d_ws, size_t ws_size, hipStream_t stream) {
    P1 a; a.x = (const char*)d_in[0]; a.wr = (const char*)d_in[1]; a.wi = (const char*)d_in[2]; a.u = (const char*)d_in[3];
    a.Are = (const u16*)d_in[4]; a.Aim = (const u16*)d_in[5]; a.Bre = (const u16*)d_in[6]; a.Bim = (const u16*)d_in[7];
    a.Cre = (const u16*)d_in[8]; a.Cim = (const u16*)d_in[9]; a.logdt = (const u16*)d_in[10]; a.ws = (char*)d_ws;
    k1<<<K1_KGEN + K1_ROW + K1_TR, 256, 0, stream>>>(a);
    P2 b; b.dskip = (const u16*)d_in[11]; b.ws = (char*)d_ws;
    k2<<<K2_CONV + K2_COL, 256, 0, stream>>>(b);
    P3 c; c.ws = (const char*)d_ws; c.out = (char*)d_out;
    k3<<<K3_ROW + K3_TR, 256, 0, stream>>>(c);
}
```

```cpp
#include <hip/hip_runtime.h>
#include <stdint.h>

#define DI __device__ __forceinline__
typedef __bf16 bf8 __attribute__((ext_vector_type(8)));
typedef __bf16 bf2 __attribute__((ext_vector_type(2)));
typedef float f4 __attribute__((ext_vector_type(4)));
typedef float f2 __attribute__((ext_vector_type(2)));
typedef unsigned u4 __attribute__((ext_vector_type(4)));
typedef unsigned u2 __attribute__((ext_vector_type(2)));
typedef short s4 __attribute__((ext_vector_type(4)));
typedef _Float16 h2 __attribute__((ext_vector_type(2)));
typedef unsigned short u16;
#define LDS3 __attribute__((address_space(3)))
#define GLB1 __attribute__((address_space(1)))

constexpr int S = 2048, D = 640, NS = 128;

constexpr size_t WS_X1 = 0;
constexpr size_t WS_X2 = WS_X1 + 88 * 32768;
constexpr size_t WS_WR = WS_X2 + 352 * 8192;
constexpr size_t WS_WI = WS_WR + 321 * 4096;
constexpr size_t WS_UT = WS_WI + 321 * 4096;
constexpr size_t WS_YT = WS_UT + 640 * 4096;
constexpr size_t WS_KR = WS_YT + 640 * 4096;
constexpr int KR_PITCH = 2080;

constexpr double PI_ = 3.14159265358979323846264338327950288;
constexpr double c_cos_small(double x) { double t = 1, s = 1, x2 = x * x; for (int k = 1; k <= 11; ++k) { t *= -x2 / ((2 * k - 1) * (2 * k)); s += t; } return s; }
constexpr double c_sin_small(double x) { double t = x, s = x, x2 = x * x; for (int k = 1; k <= 11; ++k) { t *= -x2 / ((2 * k) * (2 * k + 1)); s += t; } return s; }
constexpr double c_cos(long m, long n) {
    m %= n; if (m < 0) m += n;
    if (2 * m > n) m = n - m;
    if (4 * m > n) return -c_cos(n - 2 * m, 2 * n);
    if (8 * m > n) return c_sin_small(2 * PI_ * (double)(n - 4 * m) / (double)(4 * n));
    return c_cos_small(2 * PI_ * (double)m / (double)n);
}
constexpr double c_sin(long m, long n) { return c_cos(4 * m - n, 4 * n); }
constexpr u16 c_bf16(double v) { float f = (float)v; unsigned u = __builtin_bit_cast(unsigned, f); u += 0x7fffu + ((u >> 16) & 1u); return (u16)(u >> 16); }
constexpr u16 c_f16(double v) { return __builtin_bit_cast(u16, (_Float16)v); }

template <int N> struct CosTab { double c[N]; constexpr CosTab() : c() { for (int i = 0; i < N; ++i) c[i] = c_cos(i, N); } };
constexpr CosTab<640> C640;
constexpr CosTab<2048> C2048;
constexpr double cs640(int m) { return C640.c[((m % 640) + 640) % 640]; }
constexpr double sn640(int m) { return cs640(m - 160); }
constexpr double cs2048(int m) { return C2048.c[((m % 2048) + 2048) % 2048]; }
constexpr double sn2048(int m) { return cs2048(m - 512); }

struct alignas(16) TF20 { u16 v[2][64][8]; constexpr TF20() : v() {
    for (int c = 0; c < 2; ++c) for (int l = 0; l < 64; ++l) for (int j = 0; j < 8; ++j) { int n1 = 8 * (l >> 4) + j, k1 = l & 15;
        v[c][l][j] = (n1 < 20 && k1 <= 10) ? c_bf16(c == 0 ? cs640(32 * n1 * k1) : -sn640(32 * n1 * k1)) : (u16)0; } } };
struct alignas(16) TE32N { u16 v[2][2][64][8]; constexpr TE32N() : v() {
    for (int t = 0; t < 2; ++t) for (int c = 0; c < 2; ++c) for (int l = 0; l < 64; ++l) for (int j = 0; j < 8; ++j) { int k = 8 * (l >> 4) + j, o = 16 * t + (l & 15);
        v[t][c][l][j] = c_bf16(c == 0 ? cs2048(64 * k * o) : sn2048(64 * k * o)); } } };
struct alignas(16) TE32P { u16 v[2][2][64][8]; constexpr TE32P() : v() {
    for (int t = 0; t < 2; ++t) for (int c = 0; c < 2; ++c) for (int l = 0; l < 64; ++l) for (int j = 0; j < 8; ++j) { int k = 16 * (j >> 2) + 4 * (l >> 4) + (j & 3), o = 16 * t + (l & 15);
        v[t][c][l][j] = c_bf16(c == 0 ? cs2048(64 * k * o) : sn2048(64 * k * o)); } } };
struct alignas(16) TF64 { u16 v[4][2][2][64][8]; constexpr TF64() : v() {
    for (int w = 0; w < 4; ++w) for (int c = 0; c < 2; ++c) for (int s = 0; s < 2; ++s) for (int l = 0; l < 64; ++l) for (int j = 0; j < 8; ++j) { int k = 32 * s + 8 * (l >> 4) + j, o = 16 * w + (l & 15);
        v[w][c][s][l][j] = c_bf16(c == 0 ? cs2048(32 * k * o) : sn2048(32 * k * o)); } } };
struct alignas(16) TG20 { u16 v[2][64][8]; constexpr TG20() : v() {
    for (int t = 0; t < 2; ++t) for (int l = 0; l < 64; ++l) for (int j = 0; j < 8; ++j) { int slot = 8 * (l >> 4) + j, k1 = slot >> 1, c = slot & 1, n1 = 16 * t + (l & 15);
        double w = (k1 == 0 || k1 == 10) ? 1.0 : 2.0;
        v[t][l][j] = (k1 <= 10 && n1 < 20) ? c_bf16(w / (2048.0 * 640.0) * (c == 0 ? cs640(32 * n1 * k1) : -sn640(32 * n1 * k1))) : (u16)0; } } };
struct alignas(16) TTW640A { u16 v[64][8][2]; constexpr TTW640A() : v() {
    for (int l = 0; l < 64; ++l) for (int e = 0; e < 8; ++e) { int n2 = 8 * (l >> 4) + e, k1 = l & 15; v[l][e][0] = c_f16(cs640(n2 * k1)); v[l][e][1] = c_f16(-sn640(n2 * k1)); } } };
struct alignas(16) TTW1 { u16 v[4][64][8][2]; constexpr TTW1() : v() {
    for (int w = 0; w < 4; ++w) for (int l = 0; l < 64; ++l) for (int e = 0; e < 8; ++e) { int n2 = 16 * (e >> 2) + 4 * (l >> 4) + (e & 3), k1 = 16 * w + (l & 15);
        v[w][l][e][0] = c_f16(cs2048(n2 * k1)); v[w][l][e][1] = c_f16(-sn2048(n2 * k1)); } } };
struct alignas(16) TTW2 { u16 v[4][64][8][2]; constexpr TTW2() : v() {
    for (int w = 0; w < 4; ++w) for (int l = 0; l < 64; ++l) for (int e = 0; e < 8; ++e) { int k1 = 16 * w + 4 * (l >> 4) + (e & 3), n2 = 16 * (e >> 2) + (l & 15);
        v[w][l][e][0] = c_f16(cs2048(n2 * k1)); v[w][l][e][1] = c_f16(sn2048(n2 * k1)); } } };
struct alignas(16) TTW3 { u16 v[2][6][64][4][2]; constexpr TTW3() : v() {
    for (int t = 0; t < 2; ++t) for (int p = 0; p < 6; ++p) for (int l = 0; l < 64; ++l) for (int r = 0; r < 4; ++r) { int n2 = 16 * t + 4 * (l >> 4) + r, k1 = 2 * p + ((l & 15) >> 3);
        v[t][p][l][r][0] = c_f16(cs640(n2 * k1)); v[t][p][l][r][1] = c_f16(sn640(n2 * k1)); } } };
struct alignas(16) TGMAP { u16 v[2048]; constexpr TGMAP() : v() {
    for (int x = 0; x < 2048; ++x) { float f = (float)(2047 - x); unsigned u = __builtin_bit_cast(unsigned, f); u = (u + 0x7fffu + ((u >> 16) & 1u)) & 0xffff0000u;
        v[x] = (u16)(2 * (int)__builtin_bit_cast(float, u)); } } };
__device__ const TF20 tF20 = TF20();
__device__ const TE32N tE32N = TE32N();
__device__ const TE32P tE32P = TE32P();
__device__ const TF64 tF64 = TF64();
__device__ const TG20 tG20 = TG20();
__device__ const TTW640A tTW640A = TTW640A();
__device__ const TTW1 tTW1 = TTW1();
__device__ const TTW2 tTW2 = TTW2();
__device__ const TTW3 tTW3 = TTW3();
__device__ const TGMAP tGMAP = TGMAP();

DI f4 mfma(bf8 a, bf8 b, f4 c) { return __builtin_amdgcn_mfma_f32_16x16x32_bf16(a, b, c, 0, 0, 0); }
DI unsigned pk(float a, float b) { f2 x = {a, b}; return __builtin_bit_cast(unsigned, __builtin_convertvector(x, bf2)); }
DI bf8 pk8(f4 a, f4 b) { u4 r = {pk(a[0], a[1]), pk(a[2], a[3]), pk(b[0], b[1]), pk(b[2], b[3])}; return __builtin_bit_cast(bf8, r); }
DI bf8 neg8(bf8 a) { u4 r = __builtin_bit_cast(u4, a); r ^= 0x80008000u; return __builtin_bit_cast(bf8, r); }
DI bf8 ld8(const void* p) { return __builtin_bit_cast(bf8, *(const u4*)p); }
DI float bf2f(u16 v) { return __builtin_bit_cast(float, (unsigned)v << 16); }
DI float lo16(unsigned v) { return __builtin_bit_cast(float, v << 16); }
DI float hi16(unsigned v) { return __builtin_bit_cast(float, v & 0xffff0000u); }
DI void dma16(const void* g, void* lds) { __builtin_amdgcn_global_load_lds((const GLB1 void*)g, (LDS3 void*)lds, 16, 0, 0); }
DI s4 trd(const void* lds) { return __builtin_amdgcn_ds_read_tr16_b64_v4i16((LDS3 s4*)lds); }
DI bf8 cat8(s4 a, s4 b) { return __builtin_bit_cast(bf8, __builtin_shufflevector(a, b, 0, 1, 2, 3, 4, 5, 6, 7)); }
DI void wave_lds_fence() { __builtin_amdgcn_fence(__ATOMIC_RELEASE, "wavefront"); __builtin_amdgcn_s_waitcnt(0xC07F); __builtin_amdgcn_fence(__ATOMIC_ACQUIRE, "wavefront"); }
DI void wait_vm0() { asm volatile("s_waitcnt vmcnt(0)" ::: "memory"); }
DI void cmul_h(float& x, float& y, unsigned cs) { h2 h = __builtin_bit_cast(h2, cs); float c = (float)h[0], s = (float)h[1]; float nx = x * c - y * s, ny = y * c + x * s; x = nx; y = ny; }
DI f2 cmul(f2 a, f2 b) { f2 t = a * b[0]; f2 sw = {-a[1], a[0]}; return t + sw * b[1]; }

#define TS(ws, kid, k)
#define TS_DECL()
#define TS_FLUSH(ws, kid)
#define STAMP_BEGIN()
#define STAMP_END(ws, kid)
template <int NC>
DI void transpose_wave(const char* src, size_t sp, char* dst, size_t dp, char* lds, int lane, const char* ws, int kid) {
    TS_DECL();
    TS(ws, kid, 0);
    constexpr int CH = NC / 8;
    constexpr int RP = CH * 16;
    constexpr int IT = CH;
#pragma unroll
    for (int it = 0; it < IT; ++it) { int id = it * 64 + lane, row = id / CH, ch = id % CH; dma16(src + row * sp + ch * 16, lds + it * 1024); }
    TS(ws, kid, 1);
    wait_vm0();
    TS(ws, kid, 2);
    u4 item[IT];
    const int G = lane >> 4, q = (lane & 15) >> 2, p = lane & 3;
#pragma unroll
    for (int it = 0; it < IT; ++it) {
        int pi = it * 16 + 4 * G + p, quad = pi % (NC / 4), oct = pi / (NC / 4);
        const char* a = lds + (8 * oct + q) * RP + quad * 8;
        s4 lo = trd(a), hi = trd(a + 4 * RP);
        item[it] = __builtin_bit_cast(u4, cat8(lo, hi));
    }
    wave_lds_fence();
#pragma unroll
    for (int it = 0; it < IT; ++it) {
        int pi = it * 16 + 4 * G + q, quad = pi % (NC / 4), oct = pi / (NC / 4), col = 4 * quad + p;
        *(u4*)(lds + col * 128 + ((oct ^ (col & 7)) * 16)) = item[it];
    }
    wave_lds_fence();
    TS(ws, kid, 3);
#pragma unroll
    for (int it = 0; it < IT; ++it) { int id = it * 64 + lane, col = id >> 3, oct = id & 7;
        u4 v = *(const u4*)(lds + col * 128 + ((oct ^ (col & 7)) * 16));
        *(u4*)(dst + col * dp + oct * 16) = v; }
    TS(ws, kid, 4);
    TS_FLUSH(ws, kid);
}

constexpr int K1_KGEN = 320, K1_ROW = 256, K1_TR = 256;
constexpr int K1_LDS = 26624;

DI void k1_kgen(int b, const u16* Are, const u16* Aim, const u16* Bre, const u16* Bim, const u16* Cre, const u16* Cim, const u16* logdt, char* ws, char* lds) {
    const int tid = threadIdx.x, lane = tid & 63, w = __builtin_amdgcn_readfirstlane(tid >> 6);
    const int d = 2 * b + (w >> 1), n = 64 * (w & 1) + lane, idx = d * NS + n;
    TS_DECL();
    TS(ws, 0, 0);
    const u4* gm = (const u4*)(tGMAP.v) + 2 * (tid & 127);
    const u4 gm0 = gm[0], gm1 = gm[1];
    const float are = bf2f(Are[idx]), aim = bf2f(Aim[idx]), bre = bf2f(Bre[idx]), bim = bf2f(Bim[idx]), cre = bf2f(Cre[idx]), cim = bf2f(Cim[idx]);
    const float dt = lo16(pk(expf(bf2f(logdt[d])), 0.f));
    const float er = (dt * are) * 1.44269504088896340736f, th = (dt * aim) * 0.15915494309189533577f;
    const f2 CB = cmul(f2{cre, cim}, f2{dt * bre, dt * bim});
    auto direct = [&](float v) { float e = __builtin_amdgcn_exp2f(er * v), t = __builtin_amdgcn_fractf(th * v); return f2{e * __builtin_amdgcn_cosf(t), e * __builtin_amdgcn_sinf(t)}; };
    const f2 z1 = direct(1.f), z8 = direct(8.f), z128 = direct(128.f);
    asm volatile("" :: "v"(z1[0]), "v"(z8[0]), "v"(z128[0]));
    TS(ws, 0, 1);
    constexpr int SE = 272;
    char* tb = lds + w * (16 * SE);
    auto putQ = [&](int e, f2 v) { *(unsigned*)(tb + e * SE + lane * 4) = pk(v[0], v[1]); };
    auto putP = [&](int e, f2 v) { *(unsigned*)(tb + e * SE + lane * 4) = pk(v[0], -v[1]); };
    const char* rd = tb + (lane & 15) * SE + (lane >> 4) * 16;
    bf8 qf[4];
    {
        f2 q = z8; putQ(0, f2{1.f, 0.f}); putQ(1, q);
#pragma unroll
        for (int j = 2; j < 16; ++j) { q = cmul(q, z8); putQ(j, q); }
        wave_lds_fence();
#pragma unroll
        for (int s = 0; s < 4; ++s) qf[s] = ld8(rd + s * 64);
        wave_lds_fence();
    }
    TS(ws, 0, 2);
    f4 acc[3];
    auto tile = [&](int t) { wave_lds_fence(); f4 a = {0, 0, 0, 0};
#pragma unroll
        for (int s = 0; s < 4; ++s) a = mfma(ld8(rd + s * 64), qf[s], a);
        acc[t] = a; wave_lds_fence(); };
    f2 F[8]; F[0] = CB;
#pragma unroll
    for (int r = 1; r < 8; ++r) F[r] = cmul(F[r - 1], z1);
#pragma unroll
    for (int r = 0; r < 8; ++r) { putP(r, F[r]); putP(8 + r, cmul(F[r], z128)); }
    tile(0);
    const f2 z256 = cmul(z128, z128), z384 = cmul(z256, z128), z512 = cmul(z256, z256), z640 = cmul(z512, z128), z768 = cmul(z512, z256), z896 = cmul(z768, z128), z1024 = cmul(z512, z512);
#pragma unroll
    for (int r = 0; r < 4; ++r) { putP(r, cmul(F[2 * r], z256)); putP(4 + r, cmul(F[2 * r], z384)); }
#pragma unroll
    for (int r = 0; r < 2; ++r) { putP(8 + r, cmul(F[4 * r], z512)); putP(10 + r, cmul(F[4 * r], z640)); putP(12 + r, cmul(F[4 * r], z768)); putP(14 + r, cmul(F[4 * r], z896)); }
    tile(1);
    { f2 g = cmul(CB, z1024); putP(0, g);
#pragma unroll
      for (int i = 1; i < 9; ++i) { g = cmul(g, z128); putP(i, g); } }
    tile(2);
    TS(ws, 0, 3);
    char* red = lds + 4 * 16 * SE + (w >> 1) * 3072;
    char* kd = lds + 4 * 16 * SE + 2 * 3072 + (w >> 1) * 4352;
    if (w & 1) {
#pragma unroll
        for (int t = 0; t < 3; ++t) *(f4*)(red + t * 1024 + lane * 16) = acc[t];
    }
    __syncthreads();
    if (!(w & 1)) {
        const int g = lane >> 4, j = lane & 15;
#pragma unroll
        for (int t = 0; t < 3; ++t) { const f4 o = *(const f4*)(red + t * 1024 + lane * 16);
#pragma unroll
            for (int r = 0; r < 4; ++r) { const int i = 4 * g + r;
                int p = (t == 0) ? (i & 7) + 128 * (i >> 3) : (t == 1) ? (i < 8 ? 256 + 128 * (i >> 2) + 2 * (i & 3) : 512 + 128 * ((i - 8) >> 1) + 4 * (i & 1)) : 1024 + 128 * i;
                if (t < 2 || i < 9) *(u16*)(kd + 2 * (p + 8 * j)) = (u16)pk(acc[t][r] + o[r], 0.f); } }
    }
    __syncthreads();
    TS(ws, 0, 4);
    {
        const int t = tid & 127;
        u4 o0, o1;
#pragma unroll
        for (int k = 0; k < 4; ++k) {
            o0[k] = *(const u16*)(kd + (gm0[k] & 0xffff)) | ((unsigned)*(const u16*)(kd + (gm0[k] >> 16)) << 16);
            o1[k] = *(const u16*)(kd + (gm1[k] & 0xffff)) | ((unsigned)*(const u16*)(kd + (gm1[k] >> 16)) << 16);
        }
        char* kr = ws + WS_KR + (size_t)d * (KR_PITCH * 2);
        *(u4*)(kr + t * 32) = o0; *(u4*)(kr + t * 32 + 16) = o1;
        if (t < 4) *(u4*)(kr + 4096 + t * 16) = u4{0, 0, 0, 0};
    }
    TS(ws, 0, 5);
    TS_FLUSH(ws, 0);
}

DI void k1_row(int rb, const char* x, char* ws, char* lds) {
    const int tid = threadIdx.x, lane = tid & 63, w = __builtin_amdgcn_readfirstlane(tid >> 6);
    const int n2s = rb & 31, n1g = rb >> 5, g = lane >> 4, i = lane & 15;
    TS_DECL();
    TS(ws, 0, 0);
    char* xs = lds + w * 2560;
#pragma unroll
    for (int it = 0; it < 3; ++it) { int id = it * 64 + lane; if (id < 160) { int rr = id / 80, ch = id % 80; int s = 32 * (8 * n1g + 2 * w + rr) + n2s; dma16(x + (size_t)s * 1280 + ch * 16, xs + it * 1024); } }
    const bf8 fc = ld8(tF20.v[0][lane]), fs = ld8(tF20.v[1][lane]);
    const u4 tw0 = *(const u4*)tTW640A.v[lane][0], tw1 = *(const u4*)tTW640A.v[lane][4];
    const int kt = w & 1;
    const bf8 ec = ld8(tE32N.v[kt][0][lane]), es = ld8(tE32N.v[kt][1][lane]), en = neg8(es);
    TS(ws, 0, 1);
    wait_vm0();
    TS(ws, 0, 2);
    constexpr int SR = 128, SK = 1072;
    char* ex = lds + 10240;
    const int q = i >> 2, p = i & 3;
#pragma unroll
    for (int rr = 0; rr < 2; ++rr) {
        f4 dr[2], di[2];
#pragma unroll
        for (int t = 0; t < 2; ++t) {
            int n1a = 8 * g + q, n1b = n1a + 4; n1a = n1a < 20 ? n1a : 19; n1b = n1b < 20 ? n1b : 19;
            const char* base = xs + rr * 1280 + 16 * p + 8 * t;
            bf8 a = cat8(trd(base + 64 * n1a), trd(base + 64 * n1b));
            dr[t] = mfma(a, fc, f4{0, 0, 0, 0}); di[t] = mfma(a, fs, f4{0, 0, 0, 0});
#pragma unroll
            for (int r = 0; r < 4; ++r) { float xr = dr[t][r], xi = di[t][r]; cmul_h(xr, xi, t ? tw1[r] : tw0[r]); dr[t][r] = xr; di[t][r] = xi; }
        }
        if (i < 12) { char* o = ex + i * SK + (2 * w + rr) * SR + g * 16; *(bf8*)o = pk8(dr[0], dr[1]); *(bf8*)(o + 64) = pk8(di[0], di[1]); }
    }
    TS(ws, 0, 3);
    __syncthreads();
    TS(ws, 0, 4);
#pragma unroll
    for (int pp = 0; pp < 3; ++pp) {
        const int pr = 3 * (w >> 1) + pp;
        const char* a = ex + (2 * pr + (i >> 3)) * SK + (i & 7) * SR + g * 16;
        const bf8 ar = ld8(a), ai = ld8(a + 64);
        f4 xr = mfma(ar, ec, f4{0, 0, 0, 0}); xr = mfma(ai, es, xr);
        f4 xi = mfma(ai, ec, f4{0, 0, 0, 0}); xi = mfma(ar, en, xi);
        auto s0 = __builtin_amdgcn_permlane16_swap(pk(xr[0], xr[1]), pk(xi[0], xi[1]), false, false);
        auto s1 = __builtin_amdgcn_permlane16_swap(pk(xr[2], xr[3]), pk(xi[2], xi[3]), false, false);
        const int k1 = 2 * pr + (g >> 1), kap = 32 * k1 + 16 * kt + i;
        if (k1 <= 10) *(u4*)(ws + WS_X1 + (size_t)(kap >> 2) * 32768 + (n2s * 8 + n1g) * 128 + (kap & 3) * 32 + (g & 1) * 16) = u4{s0[0], s1[0], s0[1], s1[1]};
    }
    TS(ws, 0, 5);
    TS_FLUSH(ws, 0);
}

DI void k1_tr(int b, const char* u, const char* wr, const char* wi, char* ws, char* lds) {
    const int lane = threadIdx.x & 63, w = __builtin_amdgcn_readfirstlane(threadIdx.x >> 6);
    char* my = lds + w * 5120;
    const int xcd = b & 7, lt = b >> 3;
    if (w < 2) {
        const int cg = 2 * xcd + w;
        transpose_wave<40>(u + (size_t)lt * 64 * 1280 + cg * 80, 1280, ws + WS_UT + (size_t)cg * 40 * 4096 + lt * 128, 4096, my, lane, ws, 0);
    } else {
        const char* src = (w == 2) ? wr : wi; char* dst = ws + (w == 2 ? WS_WR : WS_WI);
        u16 last = 0;
        if (xcd == 7) last = *(const u16*)(src + (size_t)(lt * 64 + lane) * 642 + 640);
        transpose_wave<40>(src + (size_t)lt * 64 * 642 + xcd * 80, 642, dst + (size_t)xcd * 40 * 4096 + lt * 128, 4096, my, lane, ws, 0);
        if (xcd == 7) *(u16*)(dst + (size_t)320 * 4096 + lt * 128 + lane * 2) = last;
    }
}

struct P1 { const char *x, *wr, *wi, *u; const u16 *Are, *Aim, *Bre, *Bim, *Cre, *Cim, *logdt; char* ws; };
__global__ void __launch_bounds__(256, 4) k1(P1 p) {
    __shared__ __attribute__((aligned(16))) char lds[2 * 3072 + 2 * 4352 + 4 * 16 * 272 > 10240 + 12 * 1072 ? 2 * 3072 + 2 * 4352 + 4 * 16 * 272 : 10240 + 12 * 1072];
    const int b = blockIdx.x;
    STAMP_BEGIN();
    if (b < K1_KGEN) k1_kgen(b, p.Are, p.Aim, p.Bre, p.Bim, p.Cre, p.Cim, p.logdt, p.ws, lds);
    else if (b < K1_KGEN + K1_ROW) k1_row(b - K1_KGEN, p.x, p.ws, lds);
    else k1_tr(b - K1_KGEN - K1_ROW, p.u, p.wr, p.wi, p.ws, lds);
    STAMP_END(p.ws, 0);
}

constexpr int K2_CONV = 640, K2_COL = 352;
constexpr int KCP = 4160;

DI void k2_conv(int d, const u16* dskip, char* ws, char* lds) {
    const int tid = threadIdx.x, lane = tid & 63, w = __builtin_amdgcn_readfirstlane(tid >> 6), g = lane >> 4, i = lane & 15;
    TS_DECL();
    TS(ws, 1, 0);
    char* ks = lds;
    char* us = lds + 4 * KCP;
    {
        const char* kr = ws + WS_KR + (size_t)d * (KR_PITCH * 2) + w * 2 + lane * 16;
#pragma unroll
        for (int it = 0; it < 4; ++it) dma16(kr + it * 1024, ks + w * KCP + it * 1024);
        dma16(ws + WS_UT + (size_t)d * 4096 + w * 1024 + lane * 16, us + 512 + w * 1024);
        if (lane < 4) *(u4*)(ks + w * KCP + 4096 + lane * 16) = u4{0, 0, 0, 0};
        if (tid < 32) *(u4*)(us + tid * 16) = u4{0, 0, 0, 0};
    }
    const float dsk = bf2f(dskip[d]);
    TS(ws, 1, 1);
    wait_vm0();
    TS(ws, 1, 2);
    __syncthreads();
    TS(ws, 1, 3);
    f4 acc[8];
#pragma unroll
    for (int c = 0; c < 8; ++c) acc[c] = f4{0, 0, 0, 0};
    const int sh = (3 - i) & 3;
#pragma unroll 1
    for (int sg = 0; sg < 2; ++sg) {
        const int mu = 4 * w + 2 * sg;
        const int x0 = 2047 - 16 * mu - 16 * (g >> 1) + 8 * (g & 1) - i;
        const char* ap = ks + sh * KCP + 2 * (x0 - sh);
        const char* bp = us + 512 + 32 * (i - mu - (g >> 1)) + 16 * (g & 1);
        bf8 B[8];
#pragma unroll
        for (int j = 0; j < 8; ++j) B[j] = ld8(bp + 512 * j);
#pragma unroll
        for (int q = 0; q < 8; ++q) {
            const u2 a0 = *(const u2*)(ap - 512 * q), a1 = *(const u2*)(ap - 512 * q + 8);
            const bf8 A = __builtin_bit_cast(bf8, u4{a0[0], a0[1], a1[0], a1[1]});
#pragma unroll
            for (int c = q; c < 8; ++c) acc[c] = mfma(A, B[c - q], acc[c]);
        }
    }
    TS(ws, 1, 4);
    u2 uu[2];
#pragma unroll
    for (int h = 0; h < 2; ++h) uu[h] = *(const u2*)(us + 512 + 2 * (256 * (2 * w + h) + 16 * i + 4 * g));
    __syncthreads();
    TS(ws, 1, 5);
#pragma unroll
    for (int c = 0; c < 8; ++c) { const int o = c >> 1; if (o != w) { const int slot = (c > 2 * w) ? c - 2 : c; *(f4*)(lds + (w * 6 + slot) * 1024 + lane * 16) = acc[c]; } }
    __syncthreads();
#pragma unroll
    for (int h = 0; h < 2; ++h) {
        f4 y = {0, 0, 0, 0};
#pragma unroll
        for (int c = 0; c < 8; ++c) if (c == 2 * w + h) y = acc[c];
        const int c = 2 * w + h;
#pragma unroll
        for (int sw = 0; sw < 4; ++sw) if (sw != w) { const int slot = (c > 2 * sw) ? c - 2 : c; y += *(const f4*)(lds + (sw * 6 + slot) * 1024 + lane * 16); }
        const unsigned y01 = pk(y[0], y[1]), y23 = pk(y[2], y[3]);
        const unsigned s01 = pk(lo16(uu[h][0]) * dsk, hi16(uu[h][0]) * dsk), s23 = pk(lo16(uu[h][1]) * dsk, hi16(uu[h][1]) * dsk);
        const u2 o = {pk(lo16(y01) + lo16(s01), hi16(y01) + hi16(s01)), pk(lo16(y23) + lo16(s23), hi16(y23) + hi16(s23))};
        *(u2*)(ws + WS_YT + (size_t)d * 4096 + 2 * (256 * c + 16 * i + 4 * g)) = o;
    }
    TS(ws, 1, 6);
    TS_FLUSH(ws, 1);
}

DI int mixsw(int r) { return (r & 3) | ((r >> 1) & 4); }

DI void k2_col(int b, char* ws, char* lds) {
    const int xcd = b & 7, y = b >> 3, gam = ((y >> 2) << 3) | xcd, kap = 4 * gam + (y & 3);
    const int k1 = kap >> 5, k2 = kap & 31;
    if ((k1 == 0 && k2 > 16) || (k1 == 10 && k2 > 15)) return;
    const int kfull = k1 + 20 * k2; const bool mir = kfull > 320; const int kc = mir ? 640 - kfull : kfull;
    const int tid = threadIdx.x, lane = tid & 63, w = __builtin_amdgcn_readfirstlane(tid >> 6), g = lane >> 4, i = lane & 15;
    TS_DECL();
    TS(ws, 1, 0);
    char* xs = lds;
    char* wl = lds + 8192;
    char* ex = lds + 16384;
#pragma unroll
    for (int it = 0; it < 2; ++it) {
        const int slot = (2 * w + it) * 64 + lane, n2 = slot >> 4, uu = (slot & 15) ^ (n2 & 15);
        dma16(ws + WS_X1 + (size_t)gam * 32768 + (n2 * 8 + (uu >> 1)) * 128 + (kap & 3) * 32 + (uu & 1) * 16, xs + (2 * w + it) * 1024);
    }
    dma16(ws + WS_WR + (size_t)kc * 4096 + w * 1024 + lane * 16, wl + w * 1024);
    dma16(ws + WS_WI + (size_t)kc * 4096 + w * 1024 + lane * 16, wl + 4096 + w * 1024);
    const bf8 fc0 = ld8(tF64.v[w][0][0][lane]), fc1 = ld8(tF64.v[w][0][1][lane]), fs0 = ld8(tF64.v[w][1][0][lane]), fs1 = ld8(tF64.v[w][1][1][lane]);
    const bf8 ec0 = ld8(tE32P.v[0][0][lane]), es0 = ld8(tE32P.v[0][1][lane]), ec1 = ld8(tE32P.v[1][0][lane]), es1 = ld8(tE32P.v[1][1][lane]);
    const u4 ta0 = *(const u4*)tTW1.v[w][lane][0], ta1 = *(const u4*)tTW1.v[w][lane][4];
    const u4 tb0 = *(const u4*)tTW2.v[w][lane][0], tb1 = *(const u4*)tTW2.v[w][lane][4];
    TS(ws, 1, 1);
    wait_vm0();
    TS(ws, 1, 2);
    __syncthreads();
    TS(ws, 1, 3);
    const bf8 fn0 = neg8(fs0), fn1 = neg8(fs1), en0 = neg8(es0), en1 = neg8(es1);
    f4 yr[2], yi[2];
#pragma unroll
    for (int t = 0; t < 2; ++t) {
        const int n2 = 16 * t + i; const char* row = xs + n2 * 256;
        auto fr = [&](int ks, int c) { return ld8(row + ((((4 * ks + g) * 2 + c) ^ i) * 16)); };
        const bf8 r0 = fr(0, 0), r1 = fr(1, 0), i0 = fr(0, 1), i1 = fr(1, 1);
        f4 a = mfma(r0, fc0, f4{0, 0, 0, 0}); a = mfma(r1, fc1, a); a = mfma(i0, fs0, a); a = mfma(i1, fs1, a);
        f4 c = mfma(i0, fc0, f4{0, 0, 0, 0}); c = mfma(i1, fc1, c); c = mfma(r0, fn0, c); c = mfma(r1, fn1, c);
#pragma unroll
        for (int r = 0; r < 4; ++r) { float x = a[r], yv = c[r]; cmul_h(x, yv, t ? ta1[r] : ta0[r]); a[r] = x; c[r] = yv; }
        yr[t] = a; yi[t] = c;
    }
    const bf8 Yr = pk8(yr[0], yr[1]), Yi = pk8(yi[0], yi[1]);
    f4 zr[2], zi[2];
    zr[0] = mfma(es0, Yi, mfma(ec0, Yr, f4{0, 0, 0, 0})); zi[0] = mfma(en0, Yr, mfma(ec0, Yi, f4{0, 0, 0, 0}));
    zr[1] = mfma(es1, Yi, mfma(ec1, Yr, f4{0, 0, 0, 0})); zi[1] = mfma(en1, Yr, mfma(ec1, Yi, f4{0, 0, 0, 0}));
#pragma unroll
    for (int t = 0; t < 2; ++t)
#pragma unroll
        for (int r = 0; r < 4; ++r) {
            const int ks = 16 * w + i + 64 * (16 * t + 4 * g + r), pos = mir ? ((2048 - ks) & 2047) : ks;
            const float a = bf2f(*(const u16*)(wl + 2 * pos)); float bq = bf2f(*(const u16*)(wl + 4096 + 2 * pos)); if (mir) bq = -bq;
            const float x = zr[t][r], yv = zi[t][r];
            zr[t][r] = x * a - yv * bq; zi[t][r] = yv * a + x * bq;
        }
    const bf8 Zr = pk8(zr[0], zr[1]), Zi = pk8(zi[0], zi[1]);
#pragma unroll
    for (int tt = 0; tt < 2; ++tt) {
        const bf8 ec = tt ? ec1 : ec0, es = tt ? es1 : es0, en = tt ? en1 : en0;
        f4 vr = mfma(Zi, en, mfma(Zr, ec, f4{0, 0, 0, 0})), vi = mfma(Zr, es, mfma(Zi, ec, f4{0, 0, 0, 0}));
#pragma unroll
        for (int r = 0; r < 4; ++r) { float x = vr[r], yv = vi[r]; cmul_h(x, yv, tt ? tb1[r] : tb0[r]); vr[r] = x; vi[r] = yv; }
        const int row = 16 * tt + i, k = 16 * w + 4 * g;
        char* o = ex + row * 128 + (((k >> 3) ^ mixsw(row)) * 16) + (k & 7) * 2;
        *(u2*)o = u2{pk(vr[0], vr[1]), pk(vr[2], vr[3])}; *(u2*)(o + 4096) = u2{pk(vi[0], vi[1]), pk(vi[2], vi[3])};
    }
    TS(ws, 1, 4);
    __syncthreads();
    TS(ws, 1, 5);
    f4 or_[2], oi[2];
#pragma unroll
    for (int tt = 0; tt < 2; ++tt) {
        const int row = 8 * (i >> 2) + 4 * tt + (i & 3); const char* base = ex + row * 128; const int sw = mixsw(row);
        const bf8 r0 = ld8(base + ((g ^ sw) * 16)), r1 = ld8(base + (((4 + g) ^ sw) * 16)), i0 = ld8(base + 4096 + ((g ^ sw) * 16)), i1 = ld8(base + 4096 + (((4 + g) ^ sw) * 16));
        f4 a = mfma(r0, fc0, f4{0, 0, 0, 0}); a = mfma(r1, fc1, a); a = mfma(i0, fn0, a); a = mfma(i1, fn1, a);
        f4 c = mfma(i0, fc0, f4{0, 0, 0, 0}); c = mfma(i1, fc1, c); c = mfma(r0, fs0, c); c = mfma(r1, fs1, c);
        or_[tt] = a; oi[tt] = c;
    }
    u4 vr = __builtin_bit_cast(u4, pk8(or_[0], or_[1])), vi = __builtin_bit_cast(u4, pk8(oi[0], oi[1]));
    if (kfull == 0 || kfull == 320) vi = u4{0, 0, 0, 0};
    char* o = ws + WS_X2 + (size_t)kap * 8192 + g * 2048 + (16 * w + i) * 32;
    *(u4*)o = vr; *(u4*)(o + 16) = vi;
    int kap2 = -1;
    if (k1 == 0 && k2 >= 1 && k2 <= 15) kap2 = 32 - k2; else if (k1 == 10) kap2 = 320 + 31 - k2;
    TS(ws, 1, 6);
    if (kap2 >= 0) { char* o2 = ws + WS_X2 + (size_t)kap2 * 8192 + g * 2048 + (16 * w + i) * 32; *(u4*)o2 = vr; *(u4*)(o2 + 16) = vi ^ 0x80008000u; }
    TS_FLUSH(ws, 1);
}

struct P2 { const u16* dskip; char* ws; };
__global__ void __launch_bounds__(256, 4) k2(P2 p) {
    __shared__ __attribute__((aligned(16))) char lds[24576];
    const int b = blockIdx.x;
    STAMP_BEGIN();
    if (b < K2_CONV) k2_conv(b, p.dskip, p.ws, lds);
    else k2_col(b - K2_CONV, p.ws, lds);
    STAMP_END(p.ws, 1);
}

constexpr int K3_ROW = 256, K3_TR = 160;

DI void k3_row(int b, const char* ws, char* out, char* lds) {
    const int xcd = b & 7, y = b >> 3, gq = (xcd + 8 * (y >> 2)), gg = gq & 3, n1s = 4 * (gq >> 2) + (y & 3);
    const int tid = threadIdx.x, lane = tid & 63, w = __builtin_amdgcn_readfirstlane(tid >> 6), g = lane >> 4, i = lane & 15;
    TS_DECL();
    TS(ws, 2, 0);
    char* xs = lds;
    char* ex = lds + 11264;
    constexpr int SR = 2064;
#pragma unroll
    for (int it = 0; it < 3; ++it) { const int id = (w * 3 + it) * 64 + lane; if (id < 704) dma16(ws + WS_X2 + (size_t)(id >> 1) * 8192 + gg * 2048 + n1s * 32 + (id & 1) * 16, xs + (w * 3 + it) * 1024); }
    const int t = w & 1;
    const bf8 ec = ld8(tE32N.v[t][0][lane]), es = ld8(tE32N.v[t][1][lane]), en = neg8(es);
    const bf8 g0 = ld8(tG20.v[0][lane]), g1 = ld8(tG20.v[1][lane]);
    u2 tw[3][2];
#pragma unroll
    for (int pp = 0; pp < 3; ++pp) { const u4 v = *(const u4*)tTW3.v[t][3 * (w >> 1) + pp][lane][0]; tw[pp][0] = u2{v[0], v[1]}; tw[pp][1] = u2{v[2], v[3]}; }
    { const int rho = tid >> 5, n2 = tid & 31; *(u4*)(ex + rho * SR + n2 * 64 + 48) = u4{0, 0, 0, 0}; }
    TS(ws, 2, 1);
    wait_vm0();
    TS(ws, 2, 2);
    __syncthreads();
    TS(ws, 2, 3);
    const int q = i >> 2, p = i & 3;
#pragma unroll
    for (int pp = 0; pp < 3; ++pp) {
        const int pr = 3 * (w >> 1) + pp;
        int k1 = 2 * pr + (p >> 1); k1 = k1 <= 10 ? k1 : 10;
        const char* a = xs + (32 * k1 + 8 * g + q) * 32 + (p & 1) * 8;
        const bf8 xr = cat8(trd(a), trd(a + 128)), xi = cat8(trd(a + 16), trd(a + 16 + 128));
        f4 vr = mfma(en, xi, mfma(ec, xr, f4{0, 0, 0, 0})), vi = mfma(es, xr, mfma(ec, xi, f4{0, 0, 0, 0}));
        char* o = ex + (i & 7) * SR + (16 * t + 4 * g) * 64 + (2 * pr + (i >> 3)) * 4;
#pragma unroll
        for (int r = 0; r < 4; ++r) { float x = vr[r], yv = vi[r]; cmul_h(x, yv, tw[pp][r >> 1][r & 1]); *(unsigned*)(o + r * 64) = pk(x, yv); }
    }
    TS(ws, 2, 4);
    __syncthreads();
    TS(ws, 2, 5);
#pragma unroll
    for (int rr = 0; rr < 2; ++rr) {
        const int rho = 2 * w + rr;
        const bf8 a0 = ld8(ex + rho * SR + (8 * (i >> 2) + (i & 3)) * 64 + g * 16), a1 = ld8(ex + rho * SR + (8 * (i >> 2) + 4 + (i & 3)) * 64 + g * 16);
        const f4 z = {0, 0, 0, 0};
        const bf8 o0 = pk8(mfma(a0, g0, z), mfma(a1, g0, z)), o1 = pk8(mfma(a0, g1, z), mfma(a1, g1, z));
        char* orow = out + (size_t)(32 * n1s + 8 * gg + rho) * 2560;
        *(bf8*)(orow + i * 64 + g * 16) = o0;
        if (i < 4) *(bf8*)(orow + (16 + i) * 64 + g * 16) = o1;
    }
    TS(ws, 2, 6);
    TS_FLUSH(ws, 2);
}

DI void k3_tr(int b, const char* ws, char* out, char* lds) {
    const int lane = threadIdx.x & 63, w = __builtin_amdgcn_readfirstlane(threadIdx.x >> 6);
    const int id = b * 4 + w, dt = id % 10, lt = id / 10;
    transpose_wave<32>(ws + WS_YT + (size_t)dt * 64 * 4096 + lt * 64, 4096, out + (size_t)lt * 32 * 2560 + 1280 + dt * 128, 2560, lds + w * 4096, lane, ws, 2);
}

struct P3 { const char* ws; char* out; };
__global__ void __launch_bounds__(256, 4) k3(P3 p) {
    __shared__ __attribute__((aligned(16))) char lds[11264 + 8 * 2064];
    const int b = blockIdx.x;
    STAMP_BEGIN();
    if (b < K3_ROW) k3_row(b, p.ws, p.out, lds);
    else k3_tr(b - K3_ROW, p.ws, p.out, lds);
    STAMP_END((char*)p.ws, 2);
}

extern "C" void kernel_launch(void* const* d_in, const int* in_sizes, int n_in, void* d_out, int out_size, void* d_ws, size_t ws_size, hipStream_t stream) {
    P1 a; a.x = (const char*)d_in[0]; a.wr = (const char*)d_in[1]; a.wi = (const char*)d_in[2]; a.u = (const char*)d_in[3];
    a.Are = (const u16*)d_in[4]; a.Aim = (const u16*)d_in[5]; a.Bre = (const u16*)d_in[6]; a.Bim = (const u16*)d_in[7];
    a.Cre = (const u16*)d_in[8]; a.Cim = (const u16*)d_in[9]; a.logdt = (const u16*)d_in[10]; a.ws = (char*)d_ws;
    k1<<<K1_KGEN + K1_ROW + K1_TR, 256, 0, stream>>>(a);
    P2 b; b.dskip = (const u16*)d_in[11]; b.ws = (char*)d_ws;
    k2<<<K2_CONV + K2_COL, 256, 0, stream>>>(b);
    P3 c; c.ws = (const char*)d_ws; c.out = (char*)d_out;
    k3<<<K3_ROW + K3_TR, 256, 0, stream>>>(c);
}
```

```cpp
#include <hip/hip_runtime.h>
#include <stdint.h>

#define DI __device__ __forceinline__
typedef __bf16 bf8 __attribute__((ext_vector_type(8)));
typedef __bf16 bf2 __attribute__((ext_vector_type(2)));
typedef float f4 __attribute__((ext_vector_type(4)));
typedef float f2 __attribute__((ext_vector_type(2)));
typedef unsigned u4 __attribute__((ext_vector_type(4)));
typedef unsigned u2 __attribute__((ext_vector_type(2)));
typedef short s4 __attribute__((ext_vector_type(4)));
typedef _Float16 h2 __attribute__((ext_vector_type(2)));
typedef unsigned short u16;
#define LDS3 __attribute__((address_space(3)))
#define GLB1 __attribute__((address_space(1)))

constexpr int NS = 128;

constexpr int WP = 4224;
constexpr unsigned WS_X1 = 0;
constexpr unsigned WS_X2 = WS_X1 + 88 * 32768;
constexpr unsigned WS_WR = WS_X2 + 352 * 8192;
constexpr unsigned WS_WI = WS_WR + 321 * WP;
constexpr unsigned WS_UT = (WS_WI + 321 * WP + 4095) / 4096 * 4096;
constexpr unsigned WS_YT = WS_UT + 640 * 4096;
constexpr unsigned WS_SYNC = WS_YT + 640 * 4096;
constexpr unsigned WS_USED = WS_SYNC + 1024 + 5 * 65536;

constexpr double PI_ = 3.14159265358979323846264338327950288;
constexpr double c_cos_small(double x) { double t = 1, s = 1, x2 = x * x; for (int k = 1; k <= 11; ++k) { t *= -x2 / ((2 * k - 1) * (2 * k)); s += t; } return s; }
constexpr double c_sin_small(double x) { double t = x, s = x, x2 = x * x; for (int k = 1; k <= 11; ++k) { t *= -x2 / ((2 * k) * (2 * k + 1)); s += t; } return s; }
constexpr double c_cos(long m, long n) {
    m %= n; if (m < 0) m += n;
    if (2 * m > n) m = n - m;
    if (4 * m > n) return -c_cos(n - 2 * m, 2 * n);
    if (8 * m > n) return c_sin_small(2 * PI_ * (double)(n - 4 * m) / (double)(4 * n));
    return c_cos_small(2 * PI_ * (double)m / (double)n);
}
constexpr double c_sin(long m, long n) { return c_cos(4 * m - n, 4 * n); }
constexpr u16 c_bf16(double v) { float f = (float)v; unsigned u = __builtin_bit_cast(unsigned, f); u += 0x7fffu + ((u >> 16) & 1u); return (u16)(u >> 16); }
constexpr u16 c_f16(double v) { return __builtin_bit_cast(u16, (_Float16)v); }

template <int N> struct CosTab { double c[N]; constexpr CosTab() : c() { for (int i = 0; i < N; ++i) c[i] = c_cos(i, N); } };
constexpr CosTab<640> C640;
constexpr CosTab<2048> C2048;
constexpr double cs640(int m) { return C640.c[((m % 640) + 640) % 640]; }
constexpr double sn640(int m) { return cs640(m - 160); }
constexpr double cs2048(int m) { return C2048.c[((m % 2048) + 2048) % 2048]; }
constexpr double sn2048(int m) { return cs2048(m - 512); }

struct alignas(16) TF20 { u16 v[2][64][8]; constexpr TF20() : v() {
    for (int c = 0; c < 2; ++c) for (int l = 0; l < 64; ++l) for (int j = 0; j < 8; ++j) { int n1 = 8 * (l >> 4) + j, k1 = l & 15;
        v[c][l][j] = (n1 < 20 && k1 <= 10) ? c_bf16(c == 0 ? cs640(32 * n1 * k1) : -sn640(32 * n1 * k1)) : (u16)0; } } };
struct alignas(16) TE32N { u16 v[2][2][64][8]; constexpr TE32N() : v() {
    for (int t = 0; t < 2; ++t) for (int c = 0; c < 2; ++c) for (int l = 0; l < 64; ++l) for (int j = 0; j < 8; ++j) { int k = 8 * (l >> 4) + j, o = 16 * t + (l & 15);
        v[t][c][l][j] = c_bf16(c == 0 ? cs2048(64 * k * o) : sn2048(64 * k * o)); } } };
struct alignas(16) TE32P { u16 v[2][2][64][8]; constexpr TE32P() : v() {
    for (int t = 0; t < 2; ++t) for (int c = 0; c < 2; ++c) for (int l = 0; l < 64; ++l) for (int j = 0; j < 8; ++j) { int k = 16 * (j >> 2) + 4 * (l >> 4) + (j & 3), o = 16 * t + (l & 15);
        v[t][c][l][j] = c_bf16(c == 0 ? cs2048(64 * k * o) : sn2048(64 * k * o)); } } };
struct alignas(16) TF64 { u16 v[4][2][2][64][8]; constexpr TF64() : v() {
    for (int w = 0; w < 4; ++w) for (int c = 0; c < 2; ++c) for (int s = 0; s < 2; ++s) for (int l = 0; l < 64; ++l) for (int j = 0; j < 8; ++j) { int k = 32 * s + 8 * (l >> 4) + j, o = 16 * w + (l & 15);
        v[w][c][s][l][j] = c_bf16(c == 0 ? cs2048(32 * k * o) : sn2048(32 * k * o)); } } };
struct alignas(16) TG20 { u16 v[2][64][8]; constexpr TG20() : v() {
    for (int t = 0; t < 2; ++t) for (int l = 0; l < 64; ++l) for (int j = 0; j < 8; ++j) { int slot = 8 * (l >> 4) + j, k1 = slot >> 1, c = slot & 1, n1 = 16 * t + (l & 15);
        double w = (k1 == 0 || k1 == 10) ? 1.0 : 2.0;
        v[t][l][j] = (k1 <= 10 && n1 < 20) ? c_bf16(w / (2048.0 * 640.0) * (c == 0 ? cs640(32 * n1 * k1) : -sn640(32 * n1 * k1))) : (u16)0; } } };
struct alignas(16) TTW640A { u16 v[64][8][2]; constexpr TTW640A() : v() {
    for (int l = 0; l < 64; ++l) for (int e = 0; e < 8; ++e) { int n2 = 8 * (l >> 4) + e, k1 = l & 15; v[l][e][0] = c_f16(cs640(n2 * k1)); v[l][e][1] = c_f16(-sn640(n2 * k1)); } } };
struct alignas(16) TTW1 { u16 v[4][2][64][4][2]; constexpr TTW1() : v() {
    for (int w = 0; w < 4; ++w) for (int l = 0; l < 64; ++l) for (int e = 0; e < 8; ++e) { int n2 = 16 * (e >> 2) + 4 * (l >> 4) + (e & 3), k1 = 16 * w + (l & 15);
        v[w][e >> 2][l][e & 3][0] = c_f16(cs2048(n2 * k1)); v[w][e >> 2][l][e & 3][1] = c_f16(-sn2048(n2 * k1)); } } };
struct alignas(16) TTW2 { u16 v[4][2][64][4][2]; constexpr TTW2() : v() {
    for (int w = 0; w < 4; ++w) for (int l = 0; l < 64; ++l) for (int e = 0; e < 8; ++e) { int k1 = 16 * w + 4 * (l >> 4) + (e & 3), n2 = 16 * (e >> 2) + (l & 15);
        v[w][e >> 2][l][e & 3][0] = c_f16(cs2048(n2 * k1)); v[w][e >> 2][l][e & 3][1] = c_f16(sn2048(n2 * k1)); } } };
struct alignas(16) TTW3 { u16 v[2][6][64][4][2]; constexpr TTW3() : v() {
    for (int t = 0; t < 2; ++t) for (int p = 0; p < 6; ++p) for (int l = 0; l < 64; ++l) for (int r = 0; r < 4; ++r) { int n2 = 16 * t + 4 * (l >> 4) + r, k1 = 2 * p + ((l & 15) >> 3);
        v[t][p][l][r][0] = c_f16(cs640(n2 * k1)); v[t][p][l][r][1] = c_f16(sn640(n2 * k1)); } } };
constexpr int kd_off(int v) { return 2 * v + 4 * (v >> 4); }
constexpr int KD_ZERO = kd_off(2172);
struct alignas(16) TGMAP { unsigned v[5][128][4]; constexpr TGMAP() : v() {
    for (int t = 0; t < 128; ++t) for (int e = 0; e < 20; ++e) { int x = 16 * t + e; if (x >= 2048) { v[e >> 2][t][e & 3] = KD_ZERO; continue; }
        float f = (float)(2047 - x); unsigned u = __builtin_bit_cast(unsigned, f); u = (u + 0x7fffu + ((u >> 16) & 1u)) & 0xffff0000u;
        v[e >> 2][t][e & 3] = kd_off((int)__builtin_bit_cast(float, u)); } } };
struct alignas(16) TP1 { u16 v[8][64][8]; constexpr TP1() : v() { constexpr TF20 f; constexpr TTW640A t; constexpr TE32N e;
    for (int l = 0; l < 64; ++l) for (int j = 0; j < 8; ++j) { v[0][l][j] = f.v[0][l][j]; v[1][l][j] = f.v[1][l][j];
        v[2][l][j] = t.v[l][j >> 1][j & 1]; v[3][l][j] = t.v[l][4 + (j >> 1)][j & 1];
        v[4][l][j] = e.v[0][0][l][j]; v[5][l][j] = e.v[0][1][l][j]; v[6][l][j] = e.v[1][0][l][j]; v[7][l][j] = e.v[1][1][l][j]; } } };
__device__ const TP1 tP1 = TP1();
struct alignas(128) TLate { TF64 f64; TE32P e32p; TTW1 tw1; TTW2 tw2; TE32N e32n; TG20 g20; TTW3 tw3; constexpr TLate() : f64(), e32p(), tw1(), tw2(), e32n(), g20(), tw3() {} };
__device__ const TLate tLate = TLate();
#define tF64 tLate.f64
#define tE32P tLate.e32p
#define tTW1 tLate.tw1
#define tTW2 tLate.tw2
#define tE32N tLate.e32n
#define tG20 tLate.g20
#define tTW3 tLate.tw3
__device__ const TGMAP tGMAP = TGMAP();

typedef __amdgpu_buffer_rsrc_t rsrc_t;
constexpr int AUX_SC1 = 16;
DI f4 mfma(bf8 a, bf8 b, f4 c) { return __builtin_amdgcn_mfma_f32_16x16x32_bf16(a, b, c, 0, 0, 0); }
DI unsigned pk(float a, float b) { f2 x = {a, b}; return __builtin_bit_cast(unsigned, __builtin_convertvector(x, bf2)); }
DI bf8 pk8(f4 a, f4 b) { u4 r = {pk(a[0], a[1]), pk(a[2], a[3]), pk(b[0], b[1]), pk(b[2], b[3])}; return __builtin_bit_cast(bf8, r); }
DI bf8 neg8(bf8 a) { u4 r = __builtin_bit_cast(u4, a); r ^= 0x80008000u; return __builtin_bit_cast(bf8, r); }
DI bf8 ld8(const void* p) { return __builtin_bit_cast(bf8, *(const u4*)p); }
DI float bf2f(u16 v) { return __builtin_bit_cast(float, (unsigned)v << 16); }
DI float lo16(unsigned v) { return __builtin_bit_cast(float, v << 16); }
DI float hi16(unsigned v) { return __builtin_bit_cast(float, v & 0xffff0000u); }
DI rsrc_t mkrs(const void* p, unsigned n) { return __builtin_amdgcn_make_buffer_rsrc((void*)p, 0, n, 0x00020000); }
DI void dma_in(const void* g, void* lds) { __builtin_amdgcn_global_load_lds((const GLB1 void*)g, (LDS3 void*)lds, 16, 0, 0); }
DI void dma_dev(const void* g, void* lds) { __builtin_amdgcn_global_load_lds((const GLB1 void*)g, (LDS3 void*)lds, 16, 0, AUX_SC1); }
DI void dma_ws(rsrc_t rs, unsigned voff, void* lds) { __builtin_amdgcn_raw_ptr_buffer_load_lds(rs, (LDS3 void*)lds, 16, voff, 0, 0, AUX_SC1); }
DI void dma_dev_async(const void* g, void* lds) {
    const unsigned dst = __builtin_amdgcn_readfirstlane((unsigned)(size_t)(LDS3 char*)lds); unsigned keep;
    asm volatile("s_mov_b32 %0, m0\n\ts_mov_b32 m0, %2\n\ts_nop 0\n\tglobal_load_lds_dwordx4 %1, off sc1\n\ts_mov_b32 m0, %0" : "=&s"(keep) : "v"(g), "s"(dst) : "memory");
}
DI void dma_in_async(const void* g, void* lds) {
    const unsigned dst = __builtin_amdgcn_readfirstlane((unsigned)(size_t)(LDS3 char*)lds); unsigned keep;
    asm volatile("s_mov_b32 %0, m0\n\ts_mov_b32 m0, %2\n\ts_nop 0\n\tglobal_load_lds_dwordx4 %1, off\n\ts_mov_b32 m0, %0" : "=&s"(keep) : "v"(g), "s"(dst) : "memory");
}
DI void st16_dev(rsrc_t rs, unsigned voff, u4 v) { __builtin_amdgcn_raw_buffer_store_b128(v, rs, voff, 0, AUX_SC1); }
DI void st16_out(rsrc_t rs, unsigned voff, u4 v) { __builtin_amdgcn_raw_buffer_store_b128(v, rs, voff, 0, 16); }
DI s4 trd(const void* lds) { return __builtin_amdgcn_ds_read_tr16_b64_v4i16((LDS3 s4*)lds); }
DI bf8 cat8(s4 a, s4 b) { return __builtin_bit_cast(bf8, __builtin_shufflevector(a, b, 0, 1, 2, 3, 4, 5, 6, 7)); }
DI void wave_lds_fence() { __builtin_amdgcn_fence(__ATOMIC_RELEASE, "wavefront"); __builtin_amdgcn_fence(__ATOMIC_ACQUIRE, "wavefront"); }
DI void wait_vm0() { asm volatile("s_waitcnt vmcnt(0)" ::: "memory"); }
DI void cmul_h(float& x, float& y, unsigned cs) { const h2 h = __builtin_bit_cast(h2, cs);
    const float ys = __builtin_fmaf((float)h[1], y, 0.f), xs = __builtin_fmaf((float)h[1], x, 0.f);
    const float nx = __builtin_fmaf((float)h[0], x, -ys), ny = __builtin_fmaf((float)h[0], y, xs); x = nx; y = ny; }
#define PIN4(a, b, c, d) asm volatile("" : "+v"(a), "+v"(b), "+v"(c), "+v"(d))
#define PIN2(a, b) asm volatile("" : "+v"(a), "+v"(b))
struct cf { float x, y; };
DI cf cmul(cf a, cf b) { return cf{a.x * b.x - a.y * b.y, a.y * b.x + a.x * b.y}; }

constexpr int NREP = 32;
constexpr int POLL_SLEEP = 2, POLL_FAR = 16, POLL_NEAR = 8;
constexpr int PRIO_A1 = 3, PRIO_A2 = 1, PRIO_B1 = 2;
enum { F_A1 = 0, F_A2 = 1, F_B1 = 2, F_B2 = 3, F_AW = 4 };
DI unsigned read_tag(rsrc_t rs, unsigned off) { return __builtin_amdgcn_raw_buffer_load_b32(rs, WS_SYNC + off, 0, AUX_SC1) + 1u; }
DI void raw_barrier() { asm volatile("s_waitcnt lgkmcnt(0)\n\ts_barrier" ::: "memory"); }
DI void signal_flag(rsrc_t rs, int id, int blk, unsigned tag, int lane) {
    if (lane < NREP) __builtin_amdgcn_raw_buffer_store_b32(tag, rs, WS_SYNC + 1024 + id * (NREP * 1024) + lane * 1024 + blk * 4, 0, AUX_SC1);
}
DI void wait_flags(rsrc_t rs, int id, int blk, unsigned tag, int lane) {
    const unsigned off = WS_SYNC + 1024 + id * (NREP * 1024) + (blk & (NREP - 1)) * 1024 + lane * 16;
    bool ok = false;
    for (int spin = 0; spin < (1 << 18); ++spin) {
        if (!ok) { const u4 v = __builtin_amdgcn_raw_buffer_load_b128(rs, off, 0, AUX_SC1); ok = v[0] == tag && v[1] == tag && v[2] == tag && v[3] == tag; }
        const unsigned long long miss = __ballot(!ok);
        if (!miss) break;
        if (__builtin_popcountll(miss) > POLL_NEAR) __builtin_amdgcn_s_sleep(POLL_FAR); else
        __builtin_amdgcn_s_sleep(POLL_SLEEP);
        asm volatile("" ::: "memory");
    }
    asm volatile("" ::: "memory");
}
DI void wait_some(rsrc_t rs, int id, int rep, int first, int n, unsigned tag, int lane) {
    const unsigned off = WS_SYNC + 1024 + id * (NREP * 1024) + (rep & (NREP - 1)) * 1024 + (first + lane) * 4;
    bool ok = lane >= n;
    for (int spin = 0; spin < (1 << 18); ++spin) {
        if (!ok) ok = __builtin_amdgcn_raw_buffer_load_b32(rs, off, 0, AUX_SC1) == tag;
        if (__all(ok)) break;
        __builtin_amdgcn_s_sleep(POLL_SLEEP);
        asm volatile("" ::: "memory");
    }
    asm volatile("" ::: "memory");
}
DI bool last_arrival(char* c, unsigned n) {
    unsigned old = 0;
    if ((threadIdx.x & 63) == 0) old = __hip_atomic_fetch_add((LDS3 unsigned*)(LDS3 char*)c, 1u, __ATOMIC_RELAXED, __HIP_MEMORY_SCOPE_WORKGROUP);
    return __builtin_amdgcn_readfirstlane(old) == n;
}
DI void group_sync(char* c, unsigned target) {
    __builtin_amdgcn_fence(__ATOMIC_RELEASE, "wavefront"); __builtin_amdgcn_s_waitcnt(0xC07F);
    if ((threadIdx.x & 63) == 0) __hip_atomic_fetch_add((LDS3 unsigned*)(LDS3 char*)c, 1u, __ATOMIC_RELAXED, __HIP_MEMORY_SCOPE_WORKGROUP);
    for (int spin = 0; spin < (1 << 22); ++spin) { if (__builtin_amdgcn_readfirstlane(*(volatile LDS3 unsigned*)(LDS3 char*)c) >= target) break; __builtin_amdgcn_s_sleep(1); }
    __builtin_amdgcn_fence(__ATOMIC_ACQUIRE, "wavefront");
}
DI void lds_post(char* f, unsigned v) { wave_lds_fence(); __builtin_amdgcn_s_waitcnt(0xC07F); *(volatile LDS3 unsigned*)(LDS3 char*)f = v; }
DI void lds_await(char* f, unsigned v) {
    for (int spin = 0; spin < (1 << 22); ++spin) { if (__builtin_amdgcn_readfirstlane(*(volatile LDS3 unsigned*)(LDS3 char*)f) == v) break; __builtin_amdgcn_s_sleep(1); }
    __builtin_amdgcn_fence(__ATOMIC_ACQUIRE, "wavefront");
}

template <int NC, int DEV>
DI void tr_issue(const char* src, unsigned sp, char* lds, int lane) {
    constexpr int CH = NC / 8;
#pragma unroll
    for (int it = 0; it < CH; ++it) {
        const unsigned id = it * 64 + lane, row = CH == 4 ? id >> 2 : (id * 13108u) >> 16;
        const char* g = src + (row * (sp - 16 * CH) + id * 16);
        if (DEV == 1) dma_dev(g, lds + it * 1024); else if (DEV == 2) dma_in_async(g, lds + it * 1024); else dma_in(g, lds + it * 1024);
    }
}
template <int NC, class ST>
DI void tr_finish(char* lds, int lane, ST st) {
    constexpr int CH = NC / 8, RP = CH * 16;
    u4 item[CH];
    const int sg = 4 * (lane >> 4) + (lane & 3), q = (lane & 15) >> 2;
    const char* a = lds + (8 * (sg >> 1) + q) * RP + (sg & 1) * 8;
#pragma unroll
    for (int it = 0; it < CH; ++it) item[it] = __builtin_bit_cast(u4, cat8(trd(a + it * 16), trd(a + it * 16 + 4 * RP)));
    wave_lds_fence();
    const int sw = 4 * (lane >> 4) + q, c7 = 4 * (sw & 1) + (lane & 3);
    char* o = lds + c7 * 128 + (((sw >> 1) ^ c7) * 16);
#pragma unroll
    for (int it = 0; it < CH; ++it) *(u4*)(o + it * 1024) = item[it];
    wave_lds_fence();
    const char* r = lds + (lane >> 3) * 128 + (((lane & 7) ^ (lane >> 3)) * 16);
#pragma unroll
    for (int it = 0; it < CH; ++it) st(it, *(const u4*)(r + it * 1024));
}

struct Params { const char *x, *wr, *wi, *u; const u16 *Are, *Aim, *Bre, *Bim, *Cre, *Cim, *logdt, *dskip; char *out, *ws; };
DI int mixsw(int r) { return (r & 3) | ((r >> 1) & 4); }

DI void role_a(const int a, const Params& p, char* lds) {
    const int tid = threadIdx.x, lane = tid & 63, w = __builtin_amdgcn_readfirstlane(tid >> 6), g = lane >> 4, i = lane & 15;
    const rsrc_t rs = mkrs(p.ws, WS_USED);
    const unsigned tag = read_tag(rs, 256), tagB = read_tag(rs, 0);
    char* cnt = lds + 62080;
    if (tid < 8) *(unsigned*)(cnt + tid * 4) = 0;
    __builtin_amdgcn_s_setprio(PRIO_A1);
    {
        const int n2s = a & 31, n1g = a >> 5;
        char* xs = lds + w * 1280;
        { const char* row = p.x + ((32 * (8 * n1g + w) + n2s) * 1280 + lane * 16); dma_in_async(row, xs); if (lane < 16) dma_in_async(row + 1024, xs + 1024); }
        char* ct = lds + 33408;
        dma_in_async((const char*)&tP1 + w * 1024 + lane * 16, ct + w * 1024);
        const int kt = w & 1;
        wait_vm0();
        raw_barrier();
        const bf8 fc = ld8(ct + lane * 16), fs = ld8(ct + 1024 + lane * 16);
        const u4 tw0 = *(const u4*)(ct + 2048 + lane * 16), tw1 = *(const u4*)(ct + 3072 + lane * 16);
        const bf8 ec = ld8(ct + 4096 + kt * 2048 + lane * 16), es = ld8(ct + 5120 + kt * 2048 + lane * 16);
        constexpr int SR = 128, SK = 1072;
        char* ex = lds + 10240;
        const int q = i >> 2, pp = i & 3;
        {
            f4 dr[2], di[2];
#pragma unroll
            for (int t = 0; t < 2; ++t) {
                int n1a = 8 * g + q, n1b = n1a + 4; n1a = n1a < 20 ? n1a : 19; n1b = n1b < 20 ? n1b : 19;
                const char* base = xs + 16 * pp + 8 * t;
                const bf8 av = cat8(trd(base + 64 * n1a), trd(base + 64 * n1b));
                dr[t] = mfma(av, fc, f4{0, 0, 0, 0}); di[t] = mfma(av, fs, f4{0, 0, 0, 0});
#pragma unroll
                for (int r = 0; r < 4; ++r) { float x = dr[t][r], y = di[t][r]; cmul_h(x, y, t ? tw1[r] : tw0[r]); dr[t][r] = x; di[t][r] = y; }
            }
            const bf8 o0 = pk8(dr[0], dr[1]), o1 = pk8(di[0], di[1]);
            if (i < 12) { char* o = ex + i * SK + w * SR + g * 16; *(bf8*)o = o0; *(bf8*)(o + 64) = o1; }
        }
        raw_barrier();
        if (w < 6) {
            const bf8 en = neg8(es);
#pragma unroll
            for (int u2_ = 0; u2_ < 2; ++u2_) {
                const int pr = 2 * (w >> 1) + u2_;
                const char* av = ex + (2 * pr + (i >> 3)) * SK + (i & 7) * SR + g * 16;
                const bf8 ar = ld8(av), ai = ld8(av + 64);
                f4 xr = mfma(ar, ec, f4{0, 0, 0, 0}); xr = mfma(ai, es, xr);
                f4 xi = mfma(ai, ec, f4{0, 0, 0, 0}); xi = mfma(ar, en, xi);
                char* sg = lds + w * 1280; char* so = sg + (g >> 1) * 512 + i * 32 + (g & 1) * 8;
                *(u2*)so = u2{pk(xr[0], xr[1]), pk(xr[2], xr[3])}; *(u2*)(so + 16) = u2{pk(xi[0], xi[1]), pk(xi[2], xi[3])};
                wave_lds_fence();
                const u4 v = *(const u4*)(sg + lane * 16);
                wave_lds_fence();
                const int k1 = 2 * pr + (lane >> 5), kap = 32 * k1 + 16 * kt + ((lane >> 1) & 15);
                if (k1 <= 10) st16_dev(rs, WS_X1 + (kap >> 2) * 32768 + (n2s * 8 + n1g) * 128 + (kap & 3) * 32 + (lane & 1) * 16, v);
            }
            wait_vm0();
            if (last_arrival(cnt, 5)) signal_flag(rs, F_A1, a, tag, lane);
        }
    }
    {
        const int G = w >> 2, wq = w & 3;
        auto colmap = [&](int slot, int& gam, int& kap) { const int y = slot >> 3; gam = ((y >> 2) << 3) | (slot & 7); kap = 4 * gam + (y & 3);
            const int k1 = kap >> 5, k2 = kap & 31; return slot < 352 && !((k1 == 0 && k2 > 16) || (k1 == 10 && k2 > 15)); };
        int gam, kap, go, ko;
        const bool valid = colmap(a + 256 * G, gam, kap), validO = colmap(a + 256 * (G ^ 1), go, ko);
        const int k1 = kap >> 5, k2 = kap & 31;
        const int kfull = k1 + 20 * k2; const bool mir = kfull > 320; const int kc = mir ? 640 - kfull : kfull;
        char* xs = lds + G * 24576; char* wl = xs + 8192 + wq * 2048; char* ex = xs + 16384; char* gc = cnt + 12 + 4 * G;
        auto pollA1 = [&]() { wait_flags(rs, F_A1, a, tag, lane); lds_post(cnt + 24, 1);
            if (a == 0 && lane == 0) __builtin_amdgcn_raw_buffer_store_b32(tag, rs, WS_SYNC + 256, 0, AUX_SC1); };
        if (w == 0 && !valid && validO) pollA1();
        if (valid) {
            bf8 fc0 = ld8(tF64.v[wq][0][0][lane]), fc1 = ld8(tF64.v[wq][0][1][lane]), fs0 = ld8(tF64.v[wq][1][0][lane]), fs1 = ld8(tF64.v[wq][1][1][lane]);
            bf8 ec0 = ld8(tE32P.v[0][0][lane]), es0 = ld8(tE32P.v[0][1][lane]), ec1 = ld8(tE32P.v[1][0][lane]), es1 = ld8(tE32P.v[1][1][lane]);
            u4 ta0 = *(const u4*)tTW1.v[wq][0][lane], ta1 = *(const u4*)tTW1.v[wq][1][lane];
            u4 tb0 = *(const u4*)tTW2.v[wq][0][lane], tb1 = *(const u4*)tTW2.v[wq][1][lane];
            PIN4(fc0, fc1, fs0, fs1); PIN4(ec0, es0, ec1, es1); PIN4(ta0, ta1, tb0, tb1);
            bf8 fn0 = neg8(fs0), fn1 = neg8(fs1), en0 = neg8(es0), en1 = neg8(es1);
            PIN4(fn0, fn1, en0, en1);
            if (w == 0) pollA1();
            if (w == 4) lds_await(cnt + 24, 1);
            group_sync(gc, 4);
            __builtin_amdgcn_s_setprio(PRIO_A2);
#pragma unroll
            for (int it = 0; it < 2; ++it) {
                const int sl = (2 * wq + it) * 64 + lane, n2 = sl >> 4, uu = (sl & 15) ^ (n2 & 15);
                dma_ws(rs, WS_X1 + gam * 32768 + (n2 * 8 + (uu >> 1)) * 128 + (kap & 3) * 32 + (uu & 1) * 16, xs + (2 * wq + it) * 1024);
            }
            if (wq == 0) wait_flags(rs, F_AW, 2 * a + G, tagB, lane);
            wait_vm0();
            group_sync(gc, 8);
            {
                const char* wc = p.ws + (kc * WP + (lane >> 1) * 128 + (lane & 1) * 16 + (mir ? 2 * (49 - 16 * wq) : 32 * wq));
                dma_dev_async(wc + WS_WR, wl); dma_dev_async(wc + WS_WI, wl + 1024);
            }
            f4 yr[2], yi[2];
#pragma unroll
            for (int t = 0; t < 2; ++t) {
                const char* row = xs + (16 * t + i) * 256;
                auto fr = [&](int ks, int c) { return ld8(row + ((((4 * ks + g) * 2 + c) ^ i) * 16)); };
                const bf8 r0 = fr(0, 0), r1 = fr(1, 0), i0 = fr(0, 1), i1 = fr(1, 1);
                f4 av = mfma(r0, fc0, f4{0, 0, 0, 0}); av = mfma(r1, fc1, av); av = mfma(i0, fs0, av); av = mfma(i1, fs1, av);
                f4 c = mfma(i0, fc0, f4{0, 0, 0, 0}); c = mfma(i1, fc1, c); c = mfma(r0, fn0, c); c = mfma(r1, fn1, c);
#pragma unroll
                for (int r = 0; r < 4; ++r) { float x = av[r], yv = c[r]; cmul_h(x, yv, t ? ta1[r] : ta0[r]); av[r] = x; c[r] = yv; }
                yr[t] = av; yi[t] = c;
            }
            const bf8 Yr = pk8(yr[0], yr[1]), Yi = pk8(yi[0], yi[1]);
            f4 zr[2], zi[2];
            zr[0] = mfma(es0, Yi, mfma(ec0, Yr, f4{0, 0, 0, 0})); zi[0] = mfma(en0, Yr, mfma(ec0, Yi, f4{0, 0, 0, 0}));
            zr[1] = mfma(es1, Yi, mfma(ec1, Yr, f4{0, 0, 0, 0})); zi[1] = mfma(en1, Yr, mfma(ec1, Yi, f4{0, 0, 0, 0}));
            __builtin_amdgcn_sched_barrier(0);
            wait_vm0();
            __builtin_amdgcn_sched_barrier(0);
            const int q = i >> 2, pp = i & 3;
#pragma unroll
            for (int t = 0; t < 2; ++t) {
                u2 wr_, wi_;
                if (!mir) { const char* wa = wl + (16 * t + 4 * g + q) * 32 + 8 * pp;
                    wr_ = __builtin_bit_cast(u2, trd(wa)); wi_ = __builtin_bit_cast(u2, trd(wa + 1024)); }
                else { const char* wa = wl + (28 - 16 * t - 4 * g + q) * 32 + 8 * pp;
                    const u2 x = __builtin_bit_cast(u2, trd(wa)), y2 = __builtin_bit_cast(u2, trd(wa + 1024));
                    const unsigned x0 = __builtin_amdgcn_mov_dpp(x[0], 0x140, 0xf, 0xf, true), x1 = __builtin_amdgcn_mov_dpp(x[1], 0x140, 0xf, 0xf, true);
                    const unsigned y0 = __builtin_amdgcn_mov_dpp(y2[0], 0x140, 0xf, 0xf, true), y1 = __builtin_amdgcn_mov_dpp(y2[1], 0x140, 0xf, 0xf, true);
                    wr_ = u2{__builtin_amdgcn_alignbit(x1, x1, 16), __builtin_amdgcn_alignbit(x0, x0, 16)};
                    wi_ = u2{__builtin_amdgcn_alignbit(y1, y1, 16) ^ 0x80008000u, __builtin_amdgcn_alignbit(y0, y0, 16) ^ 0x80008000u}; }
#pragma unroll
                for (int r = 0; r < 4; ++r) {
                    const float wa = (r & 1) ? hi16(wr_[r >> 1]) : lo16(wr_[r >> 1]), wb = (r & 1) ? hi16(wi_[r >> 1]) : lo16(wi_[r >> 1]);
                    const float x = zr[t][r], yv = zi[t][r];
                    zr[t][r] = x * wa - yv * wb; zi[t][r] = yv * wa + x * wb;
                }
            }
            const bf8 Zr = pk8(zr[0], zr[1]), Zi = pk8(zi[0], zi[1]);
#pragma unroll
            for (int tt = 0; tt < 2; ++tt) {
                const bf8 ec = tt ? ec1 : ec0, es = tt ? es1 : es0, en = tt ? en1 : en0;
                f4 vr = mfma(Zi, en, mfma(Zr, ec, f4{0, 0, 0, 0})), vi = mfma(Zr, es, mfma(Zi, ec, f4{0, 0, 0, 0}));
#pragma unroll
                for (int r = 0; r < 4; ++r) { float x = vr[r], yv = vi[r]; cmul_h(x, yv, tt ? tb1[r] : tb0[r]); vr[r] = x; vi[r] = yv; }
                const int row = 16 * tt + i, k = 16 * wq + 4 * g;
                char* o = ex + row * 128 + (((k >> 3) ^ mixsw(row)) * 16) + (k & 7) * 2;
                *(u2*)o = u2{pk(vr[0], vr[1]), pk(vr[2], vr[3])}; *(u2*)(o + 4096) = u2{pk(vi[0], vi[1]), pk(vi[2], vi[3])};
            }
            group_sync(gc, 12);
            f4 or_[2], oi[2];
#pragma unroll
            for (int tt = 0; tt < 2; ++tt) {
                const int row = 8 * (i >> 2) + 4 * tt + (i & 3); const char* base = ex + row * 128; const int sw = mixsw(row);
                const bf8 r0 = ld8(base + ((g ^ sw) * 16)), r1 = ld8(base + (((4 + g) ^ sw) * 16)), i0 = ld8(base + 4096 + ((g ^ sw) * 16)), i1 = ld8(base + 4096 + (((4 + g) ^ sw) * 16));
                f4 av = mfma(r0, fc0, f4{0, 0, 0, 0}); av = mfma(r1, fc1, av); av = mfma(i0, fn0, av); av = mfma(i1, fn1, av);
                f4 c = mfma(i0, fc0, f4{0, 0, 0, 0}); c = mfma(i1, fc1, c); c = mfma(r0, fs0, c); c = mfma(r1, fs1, c);
                or_[tt] = av; oi[tt] = c;
            }
            const u4 vr = __builtin_bit_cast(u4, pk8(or_[0], or_[1])); u4 vi = __builtin_bit_cast(u4, pk8(oi[0], oi[1]));
            if (kfull == 0 || kfull == 320) vi = u4{0, 0, 0, 0};
            char* sg = xs + wq * 2048;
            *(u4*)(sg + lane * 32) = vr; *(u4*)(sg + lane * 32 + 16) = vi;
            wave_lds_fence();
            const u4 v0 = *(const u4*)(sg + lane * 16), v1 = *(const u4*)(sg + 1024 + lane * 16);
            const unsigned o = WS_X2 + (lane >> 5) * 2048 + wq * 512 + (lane & 31) * 16;
            st16_dev(rs, o + kap * 8192, v0); st16_dev(rs, o + kap * 8192 + 4096, v1);
            int kap2 = -1;
            if (k1 == 0 && k2 >= 1 && k2 <= 15) kap2 = 32 - k2; else if (k1 == 10) kap2 = 320 + 31 - k2;
            if (kap2 >= 0) { const unsigned m = (lane & 1) ? 0x80008000u : 0u; st16_dev(rs, o + kap2 * 8192, v0 ^ m); st16_dev(rs, o + kap2 * 8192 + 4096, v1 ^ m); }
            wait_vm0();
            if (last_arrival(cnt + 20, validO ? 7 : 3)) signal_flag(rs, F_A2, a, tag, lane);
        } else if (w == 0 && !validO) signal_flag(rs, F_A2, a, tag, lane);
    }
    {
        const int y = a >> 3, gq = (a & 7) + 8 * (y >> 2), gg = gq & 3, n1s = 4 * (gq >> 2) + (y & 3);
        char* xs = lds;
        char* ex = lds + 11264;
        constexpr int SR = 2064;
        const int t = w & 1, pw = w < 6 ? (w >> 1) : 0;
        bf8 ec = ld8(tE32N.v[t][0][lane]), es = ld8(tE32N.v[t][1][lane]);
        bf8 g0 = ld8(tG20.v[0][lane]), g1 = ld8(tG20.v[1][lane]);
        u4 tw0 = *(const u4*)tTW3.v[t][2 * pw][lane][0], tw1 = *(const u4*)tTW3.v[t][2 * pw + 1][lane][0];
        PIN4(ec, es, g0, g1); PIN2(tw0, tw1);
        bf8 en = neg8(es); asm volatile("" : "+v"(en));
        if (w == 0) { __builtin_amdgcn_s_sleep(16); wait_flags(rs, F_A2, a, tag, lane); }
        if (a == 0 && w == 7) { wait_flags(rs, F_B1, 0, tagB, lane); if (lane == 0) __builtin_amdgcn_raw_buffer_store_b32(tagB, rs, WS_SYNC, 0, AUX_SC1); }
        __syncthreads();
        if (tid < 256) *(u4*)(ex + (tid >> 5) * SR + (tid & 31) * 64 + 48) = u4{0, 0, 0, 0};
#pragma unroll
        for (int it = 0; it < 2; ++it) { const int id = (w * 2 + it) * 64 + lane; if (id < 704) dma_ws(rs, WS_X2 + (id >> 1) * 8192 + gg * 2048 + n1s * 32 + (id & 1) * 16, xs + (w * 2 + it) * 1024); }
        wait_vm0();
        __syncthreads();
        if (w < 6) {
            const int q = i >> 2, pp = i & 3;
#pragma unroll
            for (int u2_ = 0; u2_ < 2; ++u2_) {
                const int pr = 2 * pw + u2_;
                int k1 = 2 * pr + (pp >> 1); k1 = k1 <= 10 ? k1 : 10;
                const char* av = xs + (32 * k1 + 8 * g + q) * 32 + (pp & 1) * 8;
                const bf8 xr = cat8(trd(av), trd(av + 128)), xi = cat8(trd(av + 16), trd(av + 16 + 128));
                f4 vr = mfma(en, xi, mfma(ec, xr, f4{0, 0, 0, 0})), vi = mfma(es, xr, mfma(ec, xi, f4{0, 0, 0, 0}));
                char* o = ex + (i & 7) * SR + (16 * t + 4 * g) * 64 + (2 * pr + (i >> 3)) * 4;
#pragma unroll
                for (int r = 0; r < 4; ++r) { float x = vr[r], yv = vi[r]; cmul_h(x, yv, u2_ ? tw1[r] : tw0[r]); *(unsigned*)(o + r * 64) = pk(x, yv); }
            }
        }
        __syncthreads();
        {
            const bf8 a0 = ld8(ex + w * SR + (8 * (i >> 2) + (i & 3)) * 64 + g * 16), a1 = ld8(ex + w * SR + (8 * (i >> 2) + 4 + (i & 3)) * 64 + g * 16);
            const f4 z = {0, 0, 0, 0};
            const bf8 o0 = pk8(mfma(a0, g0, z), mfma(a1, g0, z)), o1 = pk8(mfma(a0, g1, z), mfma(a1, g1, z));
            const rsrc_t ro = mkrs(p.out, 2048 * 2560); const unsigned ooff = (32 * n1s + 8 * gg + w) * 2560;
            st16_out(ro, ooff + i * 64 + g * 16, __builtin_bit_cast(u4, o0));
            if (i < 4) st16_out(ro, ooff + (16 + i) * 64 + g * 16, __builtin_bit_cast(u4, o1));
        }
    }
}

constexpr int KCP = 4160, CH_U = 17408, CH_SZ = 22528;
constexpr int L_RB = 3 * CH_SZ, L_FL = L_RB + 10240, LDS_BYTES = L_FL + 128 + 1024;

struct KPar { u16 are, aim, bre, bim, cre, cim, ldt; };
DI void kgen_core(const KPar& kp, const int hf, char* kc, char* kd, char* fl, const int lane) {
    const float are = bf2f(kp.are), aim = bf2f(kp.aim), bre = bf2f(kp.bre), bim = bf2f(kp.bim), cre = bf2f(kp.cre), cim = bf2f(kp.cim);
    const float dt = lo16(pk(expf(bf2f(kp.ldt)), 0.f));
    const float er = (dt * are) * 1.44269504088896340736f, th = (dt * aim) * 0.15915494309189533577f;
    const cf CB = cmul(cf{cre, cim}, cf{dt * bre, dt * bim});
    auto direct = [&](float v) { float e = __builtin_amdgcn_exp2f(er * v), t = __builtin_amdgcn_fractf(th * v); return cf{e * __builtin_amdgcn_cosf(t), e * __builtin_amdgcn_sinf(t)}; };
    const cf z1 = direct(1.f), z8c = direct(8.f), z128 = direct(128.f);
    const cf z8 = cf{z8c.x, -z8c.y};
    constexpr int SE = 272;
    char* t0 = kc + hf * 8704 + lane * 4; char* t1 = t0 + 16 * SE;
    auto putQ = [&](char* tb, int e, cf v) { *(unsigned*)(tb + e * SE) = pk(v.x, v.y); };
    auto putP = putQ;
    const char* r0 = kc + hf * 8704 + (lane & 15) * SE + (lane >> 4) * 16; const char* r1 = r0 + 16 * SE;
    cf F[8], Q[16];
    const cf z2 = cmul(z1, z1), z4 = cmul(z2, z2);
    F[0] = CB; F[1] = cmul(CB, z1); F[2] = cmul(CB, z2); F[3] = cmul(F[1], z2);
#pragma unroll
    for (int r = 0; r < 4; ++r) F[4 + r] = cmul(F[r], z4);
    Q[0] = cf{1.f, 0.f}; Q[1] = z8; Q[2] = cmul(z8, z8); Q[3] = cmul(Q[2], z8); Q[4] = cmul(Q[2], Q[2]);
    Q[5] = cmul(Q[4], Q[1]); Q[6] = cmul(Q[4], Q[2]); Q[7] = cmul(Q[4], Q[3]); Q[8] = cmul(Q[4], Q[4]);
#pragma unroll
    for (int j = 1; j < 8; ++j) Q[8 + j] = cmul(Q[8], Q[j]);
#pragma unroll
    for (int j = 0; j < 16; ++j) putQ(t0, j, Q[j]);
#pragma unroll
    for (int r = 0; r < 8; ++r) { putP(t1, r, F[r]); putP(t1, 8 + r, cmul(F[r], z128)); }
    wave_lds_fence();
    bf8 qf[4]; f4 acc[3] = {{0, 0, 0, 0}, {0, 0, 0, 0}, {0, 0, 0, 0}};
#pragma unroll
    for (int s = 0; s < 4; ++s) { qf[s] = ld8(r0 + s * 64); acc[0] = mfma(ld8(r1 + s * 64), qf[s], acc[0]); }
    wave_lds_fence();
    const cf z256 = cmul(z128, z128), z384 = cmul(z256, z128), z512 = cmul(z256, z256), z640 = cmul(z512, z128), z768 = cmul(z512, z256), z896 = cmul(z512, z384), z1024 = cmul(z512, z512);
#pragma unroll
    for (int r = 0; r < 4; ++r) { putP(t0, r, cmul(F[2 * r], z256)); putP(t0, 4 + r, cmul(F[2 * r], z384)); }
#pragma unroll
    for (int r = 0; r < 2; ++r) { putP(t0, 8 + r, cmul(F[4 * r], z512)); putP(t0, 10 + r, cmul(F[4 * r], z640)); putP(t0, 12 + r, cmul(F[4 * r], z768)); putP(t0, 14 + r, cmul(F[4 * r], z896)); }
    { cf G[9]; G[0] = cmul(CB, z1024); G[1] = cmul(G[0], z128); G[2] = cmul(G[0], z256); G[3] = cmul(G[0], z384);
#pragma unroll
      for (int e = 0; e < 4; ++e) G[4 + e] = cmul(G[e], z512);
      G[8] = cmul(G[4], z512);
#pragma unroll
      for (int e = 0; e < 9; ++e) putP(t1, e, G[e]); }
    wave_lds_fence();
#pragma unroll
    for (int s = 0; s < 4; ++s) { acc[1] = mfma(ld8(r0 + s * 64), qf[s], acc[1]); acc[2] = mfma(ld8(r1 + s * 64), qf[s], acc[2]); }
    wave_lds_fence();
    char* mine = kc + hf * 8704 + lane * 16; const char* theirs = kc + (hf ^ 1) * 8704 + lane * 16;
    if (hf) { *(f4*)mine = acc[0]; *(f4*)(mine + 1024) = acc[2]; } else *(f4*)mine = acc[1];
    lds_post(fl + 4 * hf, 1);
    lds_await(fl + 4 * (hf ^ 1), 1);
    const int g = lane >> 4, j = lane & 15, jo = 16 * j + 4 * (j >> 1);
    if (hf) {
        const f4 y = acc[1] + *(const f4*)theirs;
        char* o = kd + jo + (g < 2 ? 576 + 288 * g : 1152 + 576 * (g - 2));
#pragma unroll
        for (int r = 0; r < 4; ++r) *(u16*)(o + (g < 2 ? 4 * r : 288 * (r >> 1) + 8 * (r & 1))) = (u16)pk(y[r], 0.f);
    } else {
        const f4 y0 = acc[0] + *(const f4*)theirs, y2 = acc[2] + *(const f4*)(theirs + 1024);
        char* o = kd + jo + 8 * (g & 1) + 288 * (g >> 1);
        *(unsigned*)o = pk(y0[0], y0[1]); *(unsigned*)(o + 4) = pk(y0[2], y0[3]);
        char* o2 = kd + 2304 + 1152 * g + jo;
#pragma unroll
        for (int r = 0; r < 4; ++r) if (4 * g + r < 9) *(u16*)(o2 + 288 * r) = (u16)pk(y2[r], 0.f);
        if (lane == 0) *(u16*)(kd + KD_ZERO) = 0;
    }
    lds_post(fl + 8 + 4 * hf, 1);
}
typedef unsigned short us2 __attribute__((ext_vector_type(2)));
DI void kgen_expand(const int hf, char* kc, const char* kd, char* fl, const int lane, const u4 (&gm)[5]) {
    lds_await(fl + 8 + 4 * (hf ^ 1), 1);
    unsigned P[10];
#pragma unroll
    for (int k = 0; k < 10; ++k) { us2 v; v[0] = *(const u16*)(kd + gm[k >> 1][2 * (k & 1)]); v[1] = k < 9 ? *(const u16*)(kd + gm[k >> 1][2 * (k & 1) + 1]) : (u16)0; P[k] = __builtin_bit_cast(unsigned, v); }
    unsigned Q[9];
#pragma unroll
    for (int k = 0; k < 9; ++k) Q[k] = __builtin_amdgcn_alignbit(P[k + 1], P[k], 16);
    char* o = kc + (64 * hf + lane) * 32;
    *(u4*)(o) = u4{P[0], P[1], P[2], P[3]}; *(u4*)(o + 16) = u4{P[4], P[5], P[6], P[7]};
    *(u4*)(o + KCP) = u4{Q[0], Q[1], Q[2], Q[3]}; *(u4*)(o + KCP + 16) = u4{Q[4], Q[5], Q[6], Q[7]};
    *(u4*)(o + 2 * KCP) = u4{P[1], P[2], P[3], P[4]}; *(u4*)(o + 2 * KCP + 16) = u4{P[5], P[6], P[7], P[8]};
    *(u4*)(o + 3 * KCP) = u4{Q[1], Q[2], Q[3], Q[4]}; *(u4*)(o + 3 * KCP + 16) = u4{Q[5], Q[6], Q[7], Q[8]};
    if (hf && lane < 16) *(u4*)(kc + (lane >> 2) * KCP + 4096 + (lane & 3) * 16) = u4{0, 0, 0, 0};
}

template <int H, int NST>
DI void conv_part(const char* kc, const char* us, const int mu0, const int lane, f4 (&acc)[4]) {
    constexpr int NQ = H ? 6 : 8;
    const int g = lane >> 4, i = lane & 15;
#pragma unroll
    for (int c = 0; c < 4; ++c) acc[c] = f4{0, 0, 0, 0};
#pragma unroll 1
    for (int sg = 0; sg < NST; ++sg) {
        const int mu = mu0 + 2 * sg;
        const char* ap = kc + (i >> 2) * KCP + 2 * (2032 - 16 * mu - 16 * (g >> 1) + 8 * (g & 1) + 4 * (i & 3)) - 512 * (NQ - 1);
        const char* bp = us + 512 + 32 * (i - mu - (g >> 1)) + 16 * (g & 1);
        bf8 A[NQ], B[NQ];
#pragma unroll
        for (int j = 0; j < NQ; ++j) { A[j] = cat8(trd(ap + 512 * (NQ - 1 - j)), trd(ap + 512 * (NQ - 1 - j) + 8)); B[j] = ld8(bp + 512 * j); }
        __builtin_amdgcn_sched_barrier(0);
#pragma unroll
        for (int k = 0; k < NQ; ++k)
#pragma unroll
            for (int c = 0; c < 4; ++c) { const int ct = H ? 2 + c : (c < 2 ? c : 4 + c); if (ct >= k) acc[c] = mfma(A[k], B[ct - k], acc[c]); }
        __builtin_amdgcn_sched_barrier(0);
    }
}
DI void conv_mfma(const char* reg, const int q, const int lane, f4 (&acc)[4], u2 (&uu)[2]) {
    if (q & 1) conv_part<1, 4>(reg, reg + CH_U, 8 * (q >> 1), lane, acc); else conv_part<0, 4>(reg, reg + CH_U, 8 * (q >> 1), lane, acc);
    const int ct0 = (q & 1) ? 2 + 2 * (q >> 1) : 6 * (q >> 1);
#pragma unroll
    for (int k = 0; k < 2; ++k) uu[k] = *(const u2*)(reg + CH_U + 512 + 2 * (256 * (ct0 + k) + 16 * (lane & 15) + 12 - 4 * (lane >> 4)));
}
DI void conv_post(char* red, const int q, const int lane, const f4 (&acc)[4]) {
    const int mh = q >> 1;
    char* o = red + q * 2048 + lane * 16;
    *(f4*)o = mh ? acc[0] : acc[2]; *(f4*)(o + 1024) = mh ? acc[1] : acc[3];
}
DI void conv_finish(const int d, char* red, const int q, const int lane, const f4 (&acc)[4], const u2 (&uu)[2], const float dsk, const rsrc_t rs) {
    const int mh = q >> 1, g = lane >> 4, i = lane & 15, ct0 = (q & 1) ? 2 + 2 * mh : 6 * mh;
    char* sg = red + (q ^ 2) * 2048;
    u2 res[2];
#pragma unroll
    for (int k = 0; k < 2; ++k) {
        const f4 y = (mh ? acc[2 + k] : acc[k]) + *(const f4*)(sg + k * 1024 + lane * 16);
        const unsigned y01 = pk(y[3], y[2]), y23 = pk(y[1], y[0]);
        const unsigned s01 = pk(lo16(uu[k][0]) * dsk, hi16(uu[k][0]) * dsk), s23 = pk(lo16(uu[k][1]) * dsk, hi16(uu[k][1]) * dsk);
        res[k] = u2{pk(lo16(y01) + lo16(s01), hi16(y01) + hi16(s01)), pk(lo16(y23) + lo16(s23), hi16(y23) + hi16(s23))};
    }
    wave_lds_fence();
    *(u2*)(sg + 32 * i + 24 - 8 * g) = res[0]; *(u2*)(sg + 512 + 32 * i + 24 - 8 * g) = res[1];
    wave_lds_fence();
    st16_dev(rs, WS_YT + d * 4096 + ct0 * 512 + lane * 16, *(const u4*)(sg + lane * 16));
}

DI void conv8_mfma(const char* reg, const int w, const int lane, f4 (&acc)[4], u2& uu) {
    if (w & 1) conv_part<1, 2>(reg, reg + CH_U, 4 * (w >> 1), lane, acc); else conv_part<0, 2>(reg, reg + CH_U, 4 * (w >> 1), lane, acc);
    const int mq = w >> 1, ct = (w & 1) ? 2 + mq : (mq < 2 ? mq : 4 + mq);
    uu = *(const u2*)(reg + CH_U + 512 + 2 * (256 * ct + 16 * (lane & 15) + 12 - 4 * (lane >> 4)));
}
DI void conv8_post(char* red, const int w, const int lane, const f4 (&acc)[4]) {
    const int mq = w >> 1;
#pragma unroll
    for (int c = 0; c < 4; ++c) if (c != mq) *(f4*)(red + (((w & 1) * 4 + mq) * 3 + (c > mq ? c - 1 : c)) * 1024 + lane * 16) = acc[c];
}
DI void conv8_finish(const int d, char* red, const int w, const int lane, const f4 (&acc)[4], const u2 uu, const float dsk, const rsrc_t rs) {
    const int mq = w >> 1, g = lane >> 4, i = lane & 15, ct = (w & 1) ? 2 + mq : (mq < 2 ? mq : 4 + mq);
    f4 y = mq == 0 ? acc[0] : mq == 1 ? acc[1] : mq == 2 ? acc[2] : acc[3];
#pragma unroll
    for (int sq = 0; sq < 4; ++sq) if (sq != mq) y += *(const f4*)(red + (((w & 1) * 4 + sq) * 3 + (mq > sq ? mq - 1 : mq)) * 1024 + lane * 16);
    const unsigned y01 = pk(y[3], y[2]), y23 = pk(y[1], y[0]);
    const unsigned s01 = pk(lo16(uu[0]) * dsk, hi16(uu[0]) * dsk), s23 = pk(lo16(uu[1]) * dsk, hi16(uu[1]) * dsk);
    const int s0 = mq ? 0 : 1;
    char* sg = red + (((w & 1) * 4 + s0) * 3 + (mq > s0 ? mq - 1 : mq)) * 1024;
    wave_lds_fence();
    *(u2*)(sg + 32 * i + 24 - 8 * g) = u2{pk(lo16(y01) + lo16(s01), hi16(y01) + hi16(s01)), pk(lo16(y23) + lo16(s23), hi16(y23) + hi16(s23))};
    wave_lds_fence();
    if (lane < 32) st16_dev(rs, WS_YT + d * 4096 + ct * 512 + lane * 16, *(const u4*)(sg + lane * 16));
}

DI void role_b(const int bb, const Params& p, char* lds) {
    const int tid = threadIdx.x, lane = tid & 63, w = __builtin_amdgcn_readfirstlane(tid >> 6);
    const rsrc_t rs = mkrs(p.ws, WS_USED);
    const int xg = bb & 7, yg = bb >> 3, nch = (yg >= 12 && yg < 28) ? 3 : 2;
    const int d0 = 80 * xg + (yg < 12 ? 2 * yg : yg < 28 ? 24 + 3 * (yg - 12) : 72 + 2 * (yg - 28));
    char* fl = lds + L_FL;
    const int kw = w - 2;
    const bool isk = kw >= 0 && kw < 2 * nch, istr = w < 2;
    KPar kp = {0, 0, 0, 0, 0, 0, 0};
    if (isk) { const int d = d0 + (kw >> 1); const unsigned idx = d * NS + 64 * (kw & 1) + lane;
        kp.are = p.Are[idx]; kp.aim = p.Aim[idx]; kp.ldt = p.logdt[d]; kp.bre = p.Bre[idx]; kp.bim = p.Bim[idx]; kp.cre = p.Cre[idx]; kp.cim = p.Cim[idx]; }
    const unsigned tag = read_tag(rs, 0);
    const int lt = bb >> 3, cg = 2 * (bb & 7) + (w & 1);
    char* trs = lds + L_RB + (w & 1) * 5120;
    if (istr) tr_issue<40, 0>(p.u + (lt * 64 * 1280 + cg * 80), 1280, trs, lane);
    if (tid < 32) *(unsigned*)(fl + tid * 4) = 0;
    raw_barrier();
    __builtin_amdgcn_s_setprio(PRIO_B1);
    if (istr) {
        wait_vm0();
        const unsigned dl = WS_UT + (cg * 40 + (lane >> 3)) * 4096 + lt * 128 + (lane & 7) * 16;
        tr_finish<40>(trs, lane, [&](int k, u4 v) { st16_dev(rs, dl + k * 8 * 4096, v); });
        const int arr = (bb >> 2) & 1, cw = 2 * (bb & 3) + (w & 1);
        const char* wsrc = arr ? p.wi : p.wr;
        wave_lds_fence();
        tr_issue<40, 2>(wsrc + (lt * 64 * 642 + cw * 80), 642, trs, lane);
        asm volatile("s_waitcnt vmcnt(5)" ::: "memory");
        if (last_arrival(fl + 48, 1)) signal_flag(rs, F_B1, (bb & 7) * 32 + (bb >> 3), tag, lane);
        __builtin_amdgcn_s_setprio(0);
        u16 last = 0, dup = 0;
        if (cw == 7) last = *(const u16*)(wsrc + (unsigned)((lt * 64 + lane) * 642 + 640));
        if (lt == 0) dup = *(const u16*)(wsrc + 2 * (cw * 40 + (lane < 40 ? lane : (cw == 7 ? 40 : 0))));
        wait_vm0();
        const unsigned wb = arr ? WS_WI : WS_WR, dw = wb + (cw * 40 + (lane >> 3)) * WP + lt * 128 + (lane & 7) * 16;
        tr_finish<40>(trs, lane, [&](int k, u4 v) { st16_dev(rs, dw + k * 8 * WP, v); });
        if (cw == 7) __builtin_amdgcn_raw_buffer_store_b16(last, rs, wb + 320 * WP + lt * 128 + lane * 2, 0, AUX_SC1);
        if (lt == 0 && (lane < 40 || (cw == 7 && lane == 40))) __builtin_amdgcn_raw_buffer_store_b16(dup, rs, wb + (cw * 40 + lane) * WP + 4096, 0, AUX_SC1);
        wait_vm0();
        if (last_arrival(fl + 52, 1)) signal_flag(rs, F_AW, bb, tag, lane);
    } else if (isk) {
        const int ch = kw >> 1, hf = kw & 1;
        { unsigned x_ = kp.are | kp.aim | kp.bre | kp.bim | kp.cre | kp.cim | kp.ldt; asm volatile("" : "+v"(x_)); }
        __builtin_amdgcn_sched_barrier(0);
        const u4* gmp = (const u4*)tGMAP.v[0][64 * hf + lane];
        u4 gm[5] = {gmp[0], gmp[128], gmp[256], gmp[384], gmp[512]};
        __builtin_amdgcn_sched_barrier(0);
        kgen_core(kp, hf, lds + ch * CH_SZ, lds + ch * CH_SZ + CH_U, fl + ch * 16, lane);
        kgen_expand(hf, lds + ch * CH_SZ, lds + ch * CH_SZ + CH_U, fl + ch * 16, lane, gm);
    }
    __builtin_amdgcn_s_setprio(0);
    if (w == 0) wait_some(rs, F_B1, yg, 32 * xg, 32, tag, lane);
    const float dskA = bf2f(p.dskip[d0 + (w >> 2)]), dskC = bf2f(p.dskip[d0 + nch - 1]);
    __syncthreads();
#pragma unroll
    for (int it = 0; it < 2; ++it) { const int id = w + 8 * it, ch = id >> 2;
        if (ch < nch) dma_ws(rs, WS_UT + (d0 + ch) * 4096 + (id & 3) * 1024 + lane * 16, lds + ch * CH_SZ + CH_U + 512 + (id & 3) * 1024); }
    if (tid < 96) *(u4*)(lds + (tid >> 5) * CH_SZ + CH_U + (tid & 31) * 16) = u4{0, 0, 0, 0};
    wait_vm0();
    __syncthreads();
    {
        f4 acc[4]; u2 uu[2];
        char* reg = lds + (w >> 2) * CH_SZ;
        conv_mfma(reg, w & 3, lane, acc, uu);
        __syncthreads();
        conv_post(reg, w & 3, lane, acc);
        __syncthreads();
        conv_finish(d0 + (w >> 2), reg, w & 3, lane, acc, uu, dskA, rs);
        if (nch == 3) {
            u2 u8;
            conv8_mfma(lds + 2 * CH_SZ, w, lane, acc, u8);
            __syncthreads();
            conv8_post(lds, w, lane, acc);
            __syncthreads();
            conv8_finish(d0 + 2, lds, w, lane, acc, u8, dskC, rs);
        }
    }
    wait_vm0();
    __syncthreads();
    if (w == 0) signal_flag(rs, F_B2, 32 * xg + yg, tag, lane);
    const int nt = bb < 128 ? 3 : 2, t0 = bb < 128 ? 3 * bb : 384 + 2 * (bb - 128);
    if (w == 0) { auto owner = [](int d) { const int x = d / 80, r = d % 80; return 32 * x + (r < 24 ? r >> 1 : r < 72 ? 12 + (r - 24) / 3 : 28 + ((r - 72) >> 1)); };
        const int o0 = owner(64 * (t0 >> 6)), o1 = owner(64 * ((t0 + nt - 1) >> 6) + 63); wait_some(rs, F_B2, bb, o0, o1 - o0 + 1, tag, lane); }
    __syncthreads();
    if (w < nt) {
        const int dt = (t0 + w) >> 6, ltt = (t0 + w) & 63;
        char* my = lds + w * 4096;
        tr_issue<32, 1>(p.ws + WS_YT + (dt * 64 * 4096 + ltt * 64), 4096, my, lane);
        wait_vm0();
        const rsrc_t ro = mkrs(p.out, 2048 * 2560); const unsigned dst = ltt * 32 * 2560 + 1280 + dt * 128 + (lane >> 3) * 2560 + (lane & 7) * 16;
        tr_finish<32>(my, lane, [&](int k, u4 v) { st16_out(ro, dst + k * 8 * 2560, v); });
    }
}

#if defined(__HIP_DEVICE_COMPILE__)
#define NO_PK_F32 __attribute__((target("no-packed-fp32-ops")))
#else
#define NO_PK_F32
#endif
constexpr int CODE_KB = 26;
NO_PK_F32 __global__ void __launch_bounds__(512, 4) fused_kernel(Params p) {
    __shared__ __attribute__((aligned(16))) char lds[LDS_BYTES];
    {
        unsigned long long sv; unsigned keep, t0, t1;
        asm volatile("s_getpc_b64 s[88:89]\n\t"
                     "s_lshr_b32 %2, %4, 3\n\ts_min_u32 %2, %2, %5\n\ts_lshl_b32 %2, %2, 10\n\t"
                     "s_and_b32 s88, s88, 0xffffff00\n\ts_add_u32 s88, s88, %2\n\ts_addc_u32 s89, s89, 0\n\t"
                     "v_cmp_gt_u32 vcc, 64, %6\n\ts_and_saveexec_b64 %0, vcc\n\t"
                     "s_mov_b32 %1, m0\n\ts_mov_b32 m0, %7\n\tv_lshlrev_b32 %3, 4, %6\n\t"
                     "global_load_lds_dwordx4 %3, s[88:89]\n\t"
                     "s_mov_b32 m0, %1\n\ts_mov_b64 exec, %0"
                     : "=&s"(sv), "=&s"(keep), "=&s"(t0), "=&v"(t1)
                     : "s"((unsigned)blockIdx.x), "n"(CODE_KB - 1), "v"((unsigned)threadIdx.x), "s"((unsigned)(size_t)(LDS3 char*)(lds + LDS_BYTES - 1024))
                     : "s88", "s89", "vcc", "scc", "memory");
    }
    if ((threadIdx.x >> 6) == 1) { const unsigned pc_ = (blockIdx.x >> 3) < sizeof(TLate) / 1024 ? (blockIdx.x >> 3) : 0;
        dma_in_async((const char*)&tLate + pc_ * 1024 + (threadIdx.x & 63) * 16, lds + LDS_BYTES - 1024); }
    if (blockIdx.x < 256) role_b(blockIdx.x, p, lds); else role_a(blockIdx.x - 256, p, lds);
}

extern "C" void kernel_launch(void* const* d_in, const int* in_sizes, int n_in, void* d_out, int out_size, void* d_ws, size_t ws_size, hipStream_t stream) {
    Params p;
    p.x = (const char*)d_in[0]; p.wr = (const char*)d_in[1]; p.wi = (const char*)d_in[2]; p.u = (const char*)d_in[3];
    p.Are = (const u16*)d_in[4]; p.Aim = (const u16*)d_in[5]; p.Bre = (const u16*)d_in[6]; p.Bim = (const u16*)d_in[7];
    p.Cre = (const u16*)d_in[8]; p.Cim = (const u16*)d_in[9]; p.logdt = (const u16*)d_in[10]; p.dskip = (const u16*)d_in[11];
    p.out = (char*)d_out; p.ws = (char*)d_ws;
    fused_kernel<<<512, 512, 0, stream>>>(p);
}
```
